```python
import torch

def _softmax(x, axis=-1):
    return torch.softmax(x, dim=axis)

def _gelu(x):
    return torch.nn.functional.gelu(x, approximate='tanh')

def _silu(x):
    return torch.nn.functional.silu(x)

def _sigmoid(x):
    return torch.sigmoid(x)

def _layernorm(x, axis=-1, eps=1e-05):
    m = x.mean(dim=axis, keepdim=True)
    v = x.var(dim=axis, keepdim=True, unbiased=False)
    return (x - m) / torch.sqrt(v + eps)

def _scatter_add(tgt, idx, src, axis=0):
    return tgt.clone().index_add_(axis, idx, src)

def _segment_sum(src, seg, n):
    out = torch.zeros((n,) + src.shape[1:], dtype=src.dtype)
    return out.index_add_(0, seg, src)

def _pad(x, pw):
    flat = tuple((v for pair in reversed(pw) for v in pair))
    return torch.nn.functional.pad(x, flat)
kernel_name = 'adaLN_cond__linear_kernel_hedgehog__moe_arch_mod__prompt'

def setup_inputs():

    _w = lambda a: (lambda t: t.to(torch.bfloat16) if t.is_floating_point() else t)(torch.as_tensor(a))

    def _A_setup():
        B, S, D, H = (1, 2048, 1024, 32)
        Dh = D // H
        return {'x': _w((lambda *s: torch.randn(*s, dtype=torch.bfloat16) * 0.05)(B, S, D)), 'wqkv': _w((lambda *s: torch.randn(*s, dtype=torch.bfloat16) * 0.4)(D, 3 * D)), 'w_phi': _w((lambda *s: torch.randn(*s, dtype=torch.bfloat16) * 0.4)(H, Dh, Dh) * Dh ** (-0.5)), 'wo': _w((lambda *s: torch.randn(*s, dtype=torch.bfloat16) * 0.05)(D, D))}
    _A_in = _A_setup()

    def _B_setup():
        B, S, D, F, C = (1, 2048, 1024, 3584, 2)
        return {'x': _w((lambda *s: torch.randn(*s, dtype=torch.bfloat16) * 0.05)(B, S, D)), 'w_r': _w((lambda *s: torch.randn(*s, dtype=torch.bfloat16) * 0.1)(D)), 'w_gate': _w((lambda *s: torch.randn(*s, dtype=torch.bfloat16) * 0.05)(D, F)), 'w_up': _w((lambda *s: torch.randn(*s, dtype=torch.bfloat16) * 0.05)(D, F)), 'w_down': _w((lambda *s: torch.randn(*s, dtype=torch.bfloat16) * 0.05)(F, D))}
    _B_in = _B_setup()
    return {'A_x': _A_in['x'], 'A_wqkv': _A_in['wqkv'], 'A_w_phi': _A_in['w_phi'], 'A_wo': _A_in['wo'], 'B_x': _B_in['x'], 'B_w_r': _B_in['w_r'], 'B_w_gate': _B_in['w_gate'], 'B_w_up': _B_in['w_up'], 'B_w_down': _B_in['w_down'], 'c': _w((lambda *s: torch.randn(*s, dtype=torch.bfloat16) * 0.2)(1, 1024) * 8.0), 'w_mod': _w((lambda *s: torch.randn(*s, dtype=torch.bfloat16) * 0.2)(1024, 6 * 1024) * 1024 ** (-0.5) * 8.0)}

def reference(A_x, A_wqkv, A_w_phi, A_wo, B_x, B_w_r, B_w_gate, B_w_up, B_w_down, c, w_mod):

    def _A_ref(x, wqkv, w_phi, wo):
        B, S, D = x.shape
        H = 32
        Dh = D // H
        qkv = (lambda x, *d: x.permute(*d))((x @ wqkv).reshape(B, S, 3, H, Dh), 2, 0, 3, 1, 4)
        q, k, v = (qkv[0], qkv[1], qkv[2])
        pq = torch.einsum('bhsd,hdf->bhsf', q, w_phi)
        pk = torch.einsum('bhsd,hdf->bhsf', k, w_phi)
        phi_q = torch.concatenate([_softmax(pq, axis=-1), _softmax(-pq, axis=-1)], axis=-1)
        phi_k = torch.concatenate([_softmax(pk, axis=-1), _softmax(-pk, axis=-1)], axis=-1)
        kv = torch.einsum('bhsf,bhsd->bhfd', phi_k, v)
        z = torch.einsum('bhsf->bhf', phi_k)
        num = torch.einsum('bhqf,bhfd->bhqd', phi_q, kv)
        den = torch.einsum('bhqf,bhf->bhq', phi_q, z)[..., None] + 1e-06
        o = num / den
        return (lambda x, *d: x.permute(*d))(o, 0, 2, 1, 3).reshape(B, S, D) @ wo

    def _B_ref(x, w_r, w_gate, w_up, w_down):
        B, S, D = x.shape
        C = 2
        r = x @ w_r
        idx = torch.argsort(-r, axis=-1)[:, :C]
        idx_e = (lambda x, n, axis: torch.repeat_interleave(x, n, dim=axis))(idx.reshape(B, C, 1), D, axis=2)
        x_sel = (lambda x, i, axis: torch.take_along_dim(x, i, dim=axis))(x, idx_e, axis=1)
        g = _silu(x_sel @ w_gate)
        u = x_sel @ w_up
        y = g * u @ w_down
        r_sel = _sigmoid((lambda x, i, axis: torch.take_along_dim(x, i, dim=axis))(r, idx, axis=1))
        y = y * r_sel.reshape(B, C, 1)
        b_off = (torch.arange(B) * S).reshape(B, 1)
        flat_idx = (idx + b_off).reshape(B * C)
        zero = (x * 0.0).reshape(B * S, D)
        upd = _scatter_add(zero, flat_idx, y.reshape(B * C, D), axis=0)
        return x + upd.reshape(B, S, D)
    x = A_x
    mod = (_silu(c) @ w_mod).reshape(c.shape[0], 6, 1, -1)
    sa, ca, ga, sb, cb, gb = (mod[:, 0].clone(), mod[:, 1].clone(), mod[:, 2].clone(), mod[:, 3].clone(), mod[:, 4].clone(), mod[:, 5].clone())
    h = _layernorm(x, axis=-1) * (1 + ca) + sa
    x = x + ga * _A_ref(h, A_wqkv, A_w_phi, A_wo)
    h = _layernorm(x, axis=-1) * (1 + cb) + sb
    x = x + gb * _B_ref(h, B_w_r, B_w_gate, B_w_up, B_w_down)
    return x
```

```cpp
#include <hip/hip_runtime.h>
#include <stdint.h>

typedef unsigned short u16;
typedef short bf16x8 __attribute__((ext_vector_type(8)));
typedef short s16x4 __attribute__((ext_vector_type(4)));
typedef float f32x2 __attribute__((ext_vector_type(2)));
typedef float f32x4 __attribute__((ext_vector_type(4)));
typedef float f32x16 __attribute__((ext_vector_type(16)));
typedef unsigned u32x4 __attribute__((ext_vector_type(4)));
typedef unsigned u32x2 __attribute__((ext_vector_type(2)));
typedef __bf16 bf16x2v __attribute__((ext_vector_type(2)));
typedef __attribute__((address_space(3))) s16x4 lds_s16x4;

#define S_ 2048
#define D_ 1024
#define H_ 32
#define F_ 3584

#define WS_MODP   0u
#define WS_STATS  (WS_MODP + 4u*6144u*4u)
#define WS_RS2    (WS_STATS + 2048u*8u)
#define WS_CNT    (WS_RS2 + 3u*2048u*4u)
#define WS_MODF   (WS_CNT + 256u)
#define WS_WR1    (WS_MODF + 6144u*4u)
#define WS_W12    (WS_WR1 + 1024u*4u)
#define WS_SEL    (WS_W12 + 64u)
#define WS_GUP    (WS_SEL + 64u)
#define WS_YP     (WS_GUP + 4u*2u*2u*3584u*4u)
#define WS_Q      (WS_YP + 16u*2u*1024u*4u)
#define WS_O      (WS_Q + 2048u*1024u*2u)
#define WS_X1     (WS_O + 2048u*1024u*2u)
#define WS_KVP    (WS_X1 + 2048u*1024u*2u)
#define WS_END    (WS_KVP + 16u*32u*2112u*4u)

__device__ __forceinline__ float bf2f(u16 u) { return __uint_as_float(((unsigned)u) << 16); }
__device__ __forceinline__ unsigned pk2(float a, float b) {
  f32x2 f = {a, b}; bf16x2v h = __builtin_convertvector(f, bf16x2v); return __builtin_bit_cast(unsigned, h);
}
__device__ __forceinline__ float rbf(float a) {
  return __uint_as_float(pk2(a, 0.f) << 16);
}
__device__ __forceinline__ float lo2f(unsigned w) { return __uint_as_float(w << 16); }
__device__ __forceinline__ float hi2f(unsigned w) { return __uint_as_float(w & 0xffff0000u); }
__device__ __forceinline__ float wave_sum(float v) {
  for (int o = 32; o > 0; o >>= 1) v += __shfl_xor(v, o);
  return v;
}
__device__ __forceinline__ float silu_f(float x) { return x / (1.f + __expf(-x)); }

__global__ __launch_bounds__(256) void k_prep(const u16* __restrict__ x, const u16* __restrict__ c,
                                              const u16* __restrict__ wmod, char* __restrict__ ws) {
  const int tid = threadIdx.x, lane = tid & 63, wid = tid >> 6;
  if (blockIdx.x < 384) {
    __shared__ float red[4][64];
    const int cg = blockIdx.x % 96, ks = blockIdx.x / 96;
    const int c8 = tid & 7, kr = tid >> 3;
    u32x4 w[8];
    float sc[8];
#pragma unroll
    for (int i = 0; i < 8; ++i) {
      const int k = ks * 256 + i * 32 + kr;
      w[i] = *reinterpret_cast<const u32x4*>(wmod + (size_t)k * 6144 + cg * 64 + c8 * 8);
      sc[i] = rbf(silu_f(bf2f(c[k])));
    }
    float acc[8] = {0, 0, 0, 0, 0, 0, 0, 0};
#pragma unroll
    for (int i = 0; i < 8; ++i)
#pragma unroll
      for (int j = 0; j < 4; ++j) { acc[2 * j] += sc[i] * lo2f(w[i][j]); acc[2 * j + 1] += sc[i] * hi2f(w[i][j]); }
#pragma unroll
    for (int j = 0; j < 8; ++j) {
      float v = acc[j];
      v += __shfl_xor(v, 8); v += __shfl_xor(v, 16); v += __shfl_xor(v, 32);
      acc[j] = v;
    }
    if (lane < 8)
#pragma unroll
      for (int j = 0; j < 8; ++j) red[wid][lane * 8 + j] = acc[j];
    __syncthreads();
    if (tid < 64) {
      float v = red[0][tid] + red[1][tid] + red[2][tid] + red[3][tid];
      reinterpret_cast<float*>(ws + WS_MODP)[ks * 6144 + cg * 64 + tid] = v;
    }
  } else {
    const int row = (blockIdx.x - 384) * 4 + wid;
    const u32x4 a = *reinterpret_cast<const u32x4*>(x + (size_t)row * D_ + lane * 8);
    const u32x4 b = *reinterpret_cast<const u32x4*>(x + (size_t)row * D_ + 512 + lane * 8);
    float s = 0.f;
#pragma unroll
    for (int j = 0; j < 4; ++j) s += lo2f(a[j]) + hi2f(a[j]) + lo2f(b[j]) + hi2f(b[j]);
    const float mean = wave_sum(s) * (1.f / D_);
    float q = 0.f;
#pragma unroll
    for (int j = 0; j < 4; ++j) {
      float d;
      d = lo2f(a[j]) - mean; q += d * d; d = hi2f(a[j]) - mean; q += d * d;
      d = lo2f(b[j]) - mean; q += d * d; d = hi2f(b[j]) - mean; q += d * d;
    }
    const float var = wave_sum(q) * (1.f / D_);
    if (lane == 0) {
      const float sd = rbf(sqrtf(rbf(rbf(var) + 1e-5f)));
      float* st = reinterpret_cast<float*>(ws + WS_STATS);
      st[row * 2] = rbf(mean);
      st[row * 2 + 1] = 1.f / sd;
      float* rs = reinterpret_cast<float*>(ws + WS_RS2);
      rs[row] = 0.f; rs[2048 + row] = 0.f; rs[4096 + row] = 0.f;
    }
    if (blockIdx.x == 384 && tid < 64) reinterpret_cast<unsigned*>(ws + WS_CNT)[tid] = 0u;
  }
}

#define A_OFF(row, ch) ((row) * 128 + ((((ch) ^ ((row) & 7))) << 4))
#define B_OFF(row, ch) ((((row) >> 3) * 6 + ((ch) >> 2)) * 512 + ((row) & 7) * 64 + ((((ch) & 3) ^ (((row) >> 2) & 3)) << 4))
#define K1_STAGE 40960
#define K1_MOD (2 * K1_STAGE)
#define K1_LDS (K1_MOD + 8192)
#define C_STRIDE 400
#define PHI_OFF (128 * C_STRIDE)
#define PHI_STRIDE 144

__global__ __launch_bounds__(512) void k_qkv(const u16* __restrict__ x, const u16* __restrict__ wqkv,
                                             const u16* __restrict__ wphi, char* __restrict__ ws) {
  extern __shared__ __attribute__((aligned(16))) char smem[];
  const int tid = threadIdx.x, lane = tid & 63, wid = tid >> 6;
  const int xcd = blockIdx.x & 7, idx = blockIdx.x >> 3;
  const int mt = (xcd & 1) * 8 + (idx & 7), hp = (xcd >> 1) * 4 + (idx >> 3);
  const int row0 = mt * 128;
  float* lmod = reinterpret_cast<float*>(smem + K1_MOD);
  {
    const float* mp = reinterpret_cast<const float*>(ws + WS_MODP);
#pragma unroll
    for (int i = tid; i < 1024; i += 512) {
      const float sa = mp[i] + mp[6144 + i] + mp[2 * 6144 + i] + mp[3 * 6144 + i];
      const float ca = mp[1024 + i] + mp[6144 + 1024 + i] + mp[2 * 6144 + 1024 + i] + mp[3 * 6144 + 1024 + i];
      lmod[i] = rbf(1.f + rbf(ca));
      lmod[1024 + i] = rbf(sa);
    }
  }
  const int sr = tid >> 3, sch = tid & 7;
  const float* st = reinterpret_cast<const float*>(ws + WS_STATS);
  const float m0 = st[(row0 + sr) * 2], r0 = st[(row0 + sr) * 2 + 1];
  const float m1 = st[(row0 + sr + 64) * 2], r1 = st[(row0 + sr + 64) * 2 + 1];
  const u16* xa0 = x + (size_t)(row0 + sr) * D_ + sch * 8;
  const u16* xa1 = xa0 + 64 * D_;
  const u16* wb = wqkv + (size_t)sr * 3072 + hp * 64 + sch * 8;

  u32x4 ga0, ga1, gb0, gb1, gb2;
  auto gload = [&](int t) {
    ga0 = *reinterpret_cast<const u32x4*>(xa0 + t * 64);
    ga1 = *reinterpret_cast<const u32x4*>(xa1 + t * 64);
    const u16* p = wb + (size_t)t * 64 * 3072;
    gb0 = *reinterpret_cast<const u32x4*>(p);
    gb1 = *reinterpret_cast<const u32x4*>(p + 1024);
    gb2 = *reinterpret_cast<const u32x4*>(p + 2048);
  };
  auto swrite = [&](int t, int buf) {
    char* base = smem + buf * K1_STAGE;
    const f32x4 a0 = *reinterpret_cast<const f32x4*>(lmod + t * 64 + sch * 8);
    const f32x4 a1 = *reinterpret_cast<const f32x4*>(lmod + t * 64 + sch * 8 + 4);
    const f32x4 s0 = *reinterpret_cast<const f32x4*>(lmod + 1024 + t * 64 + sch * 8);
    const f32x4 s1 = *reinterpret_cast<const f32x4*>(lmod + 1024 + t * 64 + sch * 8 + 4);
    const float av[8] = {a0[0], a0[1], a0[2], a0[3], a1[0], a1[1], a1[2], a1[3]};
    const float sv[8] = {s0[0], s0[1], s0[2], s0[3], s1[0], s1[1], s1[2], s1[3]};
    u32x4 h0, h1;
#pragma unroll
    for (int j = 0; j < 4; ++j) {
      h0[j] = pk2((lo2f(ga0[j]) - m0) * r0 * av[2 * j] + sv[2 * j], (hi2f(ga0[j]) - m0) * r0 * av[2 * j + 1] + sv[2 * j + 1]);
      h1[j] = pk2((lo2f(ga1[j]) - m1) * r1 * av[2 * j] + sv[2 * j], (hi2f(ga1[j]) - m1) * r1 * av[2 * j + 1] + sv[2 * j + 1]);
    }
    *reinterpret_cast<u32x4*>(base + A_OFF(sr, sch)) = h0;
    *reinterpret_cast<u32x4*>(base + A_OFF(sr + 64, sch)) = h1;
    *reinterpret_cast<u32x4*>(base + 16384 + B_OFF(sr, sch)) = gb0;
    *reinterpret_cast<u32x4*>(base + 16384 + B_OFF(sr, 8 + sch)) = gb1;
    *reinterpret_cast<u32x4*>(base + 16384 + B_OFF(sr, 16 + sch)) = gb2;
  };

  const int wm = wid & 1, wn = wid >> 1;
  const int l15 = lane & 15, lg = lane >> 4, lq = (lane & 15) >> 2, lp = lane & 3;
  f32x4 acc[4][3];
#pragma unroll
  for (int i = 0; i < 4; ++i)
#pragma unroll
    for (int j = 0; j < 3; ++j) acc[i][j] = f32x4{0.f, 0.f, 0.f, 0.f};

  gload(0);
  __syncthreads();
  swrite(0, 0);
  __syncthreads();
  for (int t = 0; t < 16; ++t) {
    if (t < 15) gload(t + 1);
    const char* base = smem + (t & 1) * K1_STAGE;
#pragma unroll
    for (int ks = 0; ks < 2; ++ks) {
      bf16x8 a[4], b[3];
#pragma unroll
      for (int mi = 0; mi < 4; ++mi) {
        const int r = wm * 64 + mi * 16 + l15;
        a[mi] = *reinterpret_cast<const bf16x8*>(base + A_OFF(r, ks * 4 + lg));
      }
#pragma unroll
      for (int ni = 0; ni < 3; ++ni) {
        const int ch = wn * 6 + ni * 2 + (lp >> 1);
        const int kr = ks * 32 + lg * 8 + lq;
        const s16x4 lo = __builtin_amdgcn_ds_read_tr16_b64_v4i16((lds_s16x4*)(base + 16384 + B_OFF(kr, ch) + 8 * (lp & 1)));
        const s16x4 hi = __builtin_amdgcn_ds_read_tr16_b64_v4i16((lds_s16x4*)(base + 16384 + B_OFF(kr + 4, ch) + 8 * (lp & 1)));
        b[ni] = __builtin_shufflevector(lo, hi, 0, 1, 2, 3, 4, 5, 6, 7);
      }
#pragma unroll
      for (int mi = 0; mi < 4; ++mi)
#pragma unroll
        for (int ni = 0; ni < 3; ++ni)
          acc[mi][ni] = __builtin_amdgcn_mfma_f32_16x16x32_bf16(b[ni], a[mi], acc[mi][ni], 0, 0, 0);
    }
    if (t < 15) swrite(t + 1, (t + 1) & 1);
    __syncthreads();
  }
#pragma unroll
  for (int mi = 0; mi < 4; ++mi)
#pragma unroll
    for (int ni = 0; ni < 3; ++ni) {
      const int m = wm * 64 + mi * 16 + l15, n = wn * 48 + ni * 16 + lg * 4;
      u32x2 v = {pk2(acc[mi][ni][0], acc[mi][ni][1]), pk2(acc[mi][ni][2], acc[mi][ni][3])};
      *reinterpret_cast<u32x2*>(smem + m * C_STRIDE + n * 2) = v;
    }
  __syncthreads();
  {
    u16* q = reinterpret_cast<u16*>(ws + WS_Q);
#pragma unroll
    for (int i = 0; i < 2; ++i) {
      const int r = sr + i * 64;
      const u32x4 v = *reinterpret_cast<const u32x4*>(smem + r * C_STRIDE + sch * 16);
      *reinterpret_cast<u32x4*>(q + (size_t)(row0 + r) * D_ + hp * 64 + sch * 8) = v;
    }
  }
  const int l31 = lane & 31, lh = lane >> 5;
  {
    const int hh = wid & 1, rt = wid >> 1;
    const u16* wp = wphi + (size_t)(hp * 2 + hh) * 1024;
    f32x16 xp;
#pragma unroll
    for (int i = 0; i < 16; ++i) xp[i] = 0.f;
#pragma unroll
    for (int s = 0; s < 2; ++s) {
      bf16x8 a;
#pragma unroll
      for (int j = 0; j < 8; ++j) a[j] = (short)wp[(16 * s + 8 * lh + j) * 32 + l31];
      const bf16x8 b = *reinterpret_cast<const bf16x8*>(smem + (rt * 32 + l31) * C_STRIDE + (64 + hh * 32 + 16 * s + 8 * lh) * 2);
      xp = __builtin_amdgcn_mfma_f32_32x32x16_bf16(a, b, xp, 0, 0, 0);
    }
    float mx = -3.0e38f, mn = 3.0e38f;
#pragma unroll
    for (int i = 0; i < 16; ++i) { xp[i] = rbf(xp[i]); mx = fmaxf(mx, xp[i]); mn = fminf(mn, xp[i]); }
    mx = fmaxf(mx, __shfl_xor(mx, 32)); mn = fminf(mn, __shfl_xor(mn, 32));
    float ep[16], en[16], sp = 0.f, sn = 0.f;
#pragma unroll
    for (int i = 0; i < 16; ++i) { ep[i] = __expf(xp[i] - mx); sp += ep[i]; en[i] = __expf(mn - xp[i]); sn += en[i]; }
    sp += __shfl_xor(sp, 32); sn += __shfl_xor(sn, 32);
    const float ip = 1.f / sp, in_ = 1.f / sn;
    char* ph = smem + PHI_OFF + (hh * 128 + rt * 32 + l31) * PHI_STRIDE;
#pragma unroll
    for (int g = 0; g < 4; ++g) {
      u32x2 v = {pk2(ep[4 * g] * ip, ep[4 * g + 1] * ip), pk2(ep[4 * g + 2] * ip, ep[4 * g + 3] * ip)};
      *reinterpret_cast<u32x2*>(ph + (8 * g + 4 * lh) * 2) = v;
      u32x2 w = {pk2(en[4 * g] * in_, en[4 * g + 1] * in_), pk2(en[4 * g + 2] * in_, en[4 * g + 3] * in_)};
      *reinterpret_cast<u32x2*>(ph + 64 + (8 * g + 4 * lh) * 2) = w;
    }
  }
  __syncthreads();
  if (wid < 4) {
    const int hh = wid & 1, ft = wid >> 1;
    const int blk = (lane >> 4) & 1;
    f32x16 kv, zz;
#pragma unroll
    for (int i = 0; i < 16; ++i) { kv[i] = 0.f; zz[i] = 0.f; }
    bf16x8 ones;
#pragma unroll
    for (int j = 0; j < 8; ++j) ones[j] = (short)0x3f80;
#pragma unroll
    for (int s = 0; s < 8; ++s) {
      const char* pa = smem + PHI_OFF + (hh * 128 + 16 * s + 8 * lh + lq) * PHI_STRIDE + (ft * 32 + 16 * blk + 4 * lp) * 2;
      const s16x4 alo = __builtin_amdgcn_ds_read_tr16_b64_v4i16((lds_s16x4*)pa);
      const s16x4 ahi = __builtin_amdgcn_ds_read_tr16_b64_v4i16((lds_s16x4*)(pa + 4 * PHI_STRIDE));
      const bf16x8 a = __builtin_shufflevector(alo, ahi, 0, 1, 2, 3, 4, 5, 6, 7);
      const char* pb = smem + (16 * s + 8 * lh + lq) * C_STRIDE + (128 + hh * 32 + 16 * blk + 4 * lp) * 2;
      const s16x4 blo = __builtin_amdgcn_ds_read_tr16_b64_v4i16((lds_s16x4*)pb);
      const s16x4 bhi = __builtin_amdgcn_ds_read_tr16_b64_v4i16((lds_s16x4*)(pb + 4 * C_STRIDE));
      const bf16x8 b = __builtin_shufflevector(blo, bhi, 0, 1, 2, 3, 4, 5, 6, 7);
      kv = __builtin_amdgcn_mfma_f32_32x32x16_bf16(a, b, kv, 0, 0, 0);
      zz = __builtin_amdgcn_mfma_f32_32x32x16_bf16(a, ones, zz, 0, 0, 0);
    }
    float* kp = reinterpret_cast<float*>(ws + WS_KVP) + (size_t)(mt * 32 + hp * 2 + hh) * 2112;
#pragma unroll
    for (int i = 0; i < 16; ++i) {
      const int f = ft * 32 + (i & 3) + 8 * (i >> 2) + 4 * lh;
      kp[f * 32 + l31] = kv[i];
      if (l31 == 0) kp[2048 + f] = zz[i];
    }
  }
}

__global__ __launch_bounds__(256) void k_o(const u16* __restrict__ wphi, const u16* __restrict__ wr, char* __restrict__ ws) {
  __shared__ __attribute__((aligned(16))) u16 kvt[32 * 64 + 64];
  __shared__ float zs[64];
  __shared__ float red[8];
  const int tid = threadIdx.x, lane = tid & 63, wid = tid >> 6;
  const int h = blockIdx.x & 31, rc = blockIdx.x >> 5;
  const float* kp = reinterpret_cast<const float*>(ws + WS_KVP) + (size_t)h * 2112;
  for (int i = tid; i < 2112; i += 256) {
    float s = 0.f;
#pragma unroll
    for (int m = 0; m < 16; ++m) s += kp[(size_t)m * 32 * 2112 + i];
    if (i < 2048) { const int f = i >> 5, d = i & 31; kvt[d * 64 + f] = (u16)(pk2(s, 0.f) & 0xffff); }
    else zs[i - 2048] = rbf(s);
  }
  if (blockIdx.x == 0) {
    const float* mp = reinterpret_cast<const float*>(ws + WS_MODP);
    float* mf = reinterpret_cast<float*>(ws + WS_MODF);
    float* wr1 = reinterpret_cast<float*>(ws + WS_WR1);
    float s1 = 0.f, s2 = 0.f;
    for (int i = tid; i < 1024; i += 256) {
#pragma unroll
      for (int q = 0; q < 6; ++q)
        mf[q * 1024 + i] = rbf(mp[q * 1024 + i] + mp[6144 + q * 1024 + i] + mp[2 * 6144 + q * 1024 + i] + mp[3 * 6144 + q * 1024 + i]);
      const float sb = rbf(mp[3072 + i] + mp[6144 + 3072 + i] + mp[2 * 6144 + 3072 + i] + mp[3 * 6144 + 3072 + i]);
      const float cb = rbf(mp[4096 + i] + mp[6144 + 4096 + i] + mp[2 * 6144 + 4096 + i] + mp[3 * 6144 + 4096 + i]);
      const float w = bf2f(wr[i]);
      const float a = rbf(1.f + cb) * w;
      wr1[i] = a; s1 += a; s2 += sb * w;
    }
    s1 = wave_sum(s1); s2 = wave_sum(s2);
    if (lane == 0) { red[wid] = s1; red[4 + wid] = s2; }
    __syncthreads();
    if (tid == 0) {
      float* w12 = reinterpret_cast<float*>(ws + WS_W12);
      w12[0] = red[0] + red[1] + red[2] + red[3];
      w12[1] = red[4] + red[5] + red[6] + red[7];
    }
  }
  __syncthreads();
  const int l31 = lane & 31, lh = lane >> 5;
  const u16* wp = wphi + (size_t)h * 1024;
  bf16x8 wa[2];
#pragma unroll
  for (int s = 0; s < 2; ++s)
#pragma unroll
    for (int j = 0; j < 8; ++j) wa[s][j] = (short)wp[(16 * s + 8 * lh + j) * 32 + l31];
  bf16x8 ka[4];
#pragma unroll
  for (int s = 0; s < 4; ++s)
#pragma unroll
    for (int j = 0; j < 8; ++j) {
      const int f = (s >> 1) * 32 + 16 * (s & 1) + 8 * (j >> 2) + 4 * lh + (j & 3);
      ka[s][j] = (short)kvt[l31 * 64 + f];
    }
  float zp[16], zn[16];
#pragma unroll
  for (int i = 0; i < 16; ++i) { const int f = (i & 3) + 8 * (i >> 2) + 4 * lh; zp[i] = zs[f]; zn[i] = zs[32 + f]; }
  const u16* q = reinterpret_cast<const u16*>(ws + WS_Q);
  u16* o = reinterpret_cast<u16*>(ws + WS_O);
#pragma unroll 1
  for (int rt = 0; rt < 2; ++rt) {
    const int row = rc * 256 + wid * 64 + rt * 32 + l31;
    f32x16 xp;
#pragma unroll
    for (int i = 0; i < 16; ++i) xp[i] = 0.f;
#pragma unroll
    for (int s = 0; s < 2; ++s) {
      const bf16x8 b = *reinterpret_cast<const bf16x8*>(q + (size_t)row * D_ + h * 32 + 16 * s + 8 * lh);
      xp = __builtin_amdgcn_mfma_f32_32x32x16_bf16(wa[s], b, xp, 0, 0, 0);
    }
    float mx = -3.0e38f, mn = 3.0e38f;
#pragma unroll
    for (int i = 0; i < 16; ++i) { xp[i] = rbf(xp[i]); mx = fmaxf(mx, xp[i]); mn = fminf(mn, xp[i]); }
    mx = fmaxf(mx, __shfl_xor(mx, 32)); mn = fminf(mn, __shfl_xor(mn, 32));
    float ep[16], en[16], sp = 0.f, sn = 0.f;
#pragma unroll
    for (int i = 0; i < 16; ++i) { ep[i] = __expf(xp[i] - mx); sp += ep[i]; en[i] = __expf(mn - xp[i]); sn += en[i]; }
    sp += __shfl_xor(sp, 32); sn += __shfl_xor(sn, 32);
    const float ip = 1.f / sp, in_ = 1.f / sn;
    float den = 0.f;
    bf16x8 pb[4];
#pragma unroll
    for (int s = 0; s < 2; ++s) {
      u32x4 wpk, wnk;
#pragma unroll
      for (int j = 0; j < 4; ++j) {
        wpk[j] = pk2(ep[8 * s + 2 * j] * ip, ep[8 * s + 2 * j + 1] * ip);
        wnk[j] = pk2(en[8 * s + 2 * j] * in_, en[8 * s + 2 * j + 1] * in_);
        den += lo2f(wpk[j]) * zp[8 * s + 2 * j] + hi2f(wpk[j]) * zp[8 * s + 2 * j + 1];
        den += lo2f(wnk[j]) * zn[8 * s + 2 * j] + hi2f(wnk[j]) * zn[8 * s + 2 * j + 1];
      }
      pb[s] = __builtin_bit_cast(bf16x8, wpk);
      pb[2 + s] = __builtin_bit_cast(bf16x8, wnk);
    }
    den += __shfl_xor(den, 32);
    den = rbf(rbf(den) + 1e-6f);
    f32x16 num;
#pragma unroll
    for (int i = 0; i < 16; ++i) num[i] = 0.f;
#pragma unroll
    for (int s = 0; s < 4; ++s) num = __builtin_amdgcn_mfma_f32_32x32x16_bf16(ka[s], pb[s], num, 0, 0, 0);
#pragma unroll
    for (int g = 0; g < 4; ++g) {
      u32x2 v = {pk2(rbf(num[4 * g]) / den, rbf(num[4 * g + 1]) / den), pk2(rbf(num[4 * g + 2]) / den, rbf(num[4 * g + 3]) / den)};
      *reinterpret_cast<u32x2*>(o + (size_t)row * D_ + h * 32 + 8 * g + 4 * lh) = v;
    }
  }
}

#define B3_OFF(row, ch) ((((row) >> 3) * 2 + ((ch) >> 2)) * 512 + ((row) & 7) * 64 + ((((ch) & 3) ^ (((row) >> 2) & 3)) << 4))
#define K3_STAGE 24576
__global__ __launch_bounds__(512) void k_wo(const u16* __restrict__ x, const u16* __restrict__ wo, char* __restrict__ ws) {
  __shared__ __attribute__((aligned(16))) char smem[2 * K3_STAGE];
  const int tid = threadIdx.x, lane = tid & 63, wid = tid >> 6;
  const int xcd = blockIdx.x & 7, idx = blockIdx.x >> 3;
  const int mt = (xcd & 3) * 4 + (idx & 3), nt = (xcd >> 2) * 8 + (idx >> 2);
  const int row0 = mt * 128, col0 = nt * 64;
  const u16* o = reinterpret_cast<const u16*>(ws + WS_O);
  const int sr = tid >> 3, sch = tid & 7;
  const u16* pa0 = o + (size_t)(row0 + sr) * D_ + sch * 8;
  const u16* pa1 = pa0 + 64 * D_;
  const u16* pb = wo + (size_t)sr * D_ + col0 + sch * 8;
  u32x4 ga0, ga1, gb0;
  auto gload = [&](int t) {
    ga0 = *reinterpret_cast<const u32x4*>(pa0 + t * 64);
    ga1 = *reinterpret_cast<const u32x4*>(pa1 + t * 64);
    gb0 = *reinterpret_cast<const u32x4*>(pb + (size_t)t * 64 * D_);
  };
  auto swrite = [&](int buf) {
    char* base = smem + buf * K3_STAGE;
    *reinterpret_cast<u32x4*>(base + A_OFF(sr, sch)) = ga0;
    *reinterpret_cast<u32x4*>(base + A_OFF(sr + 64, sch)) = ga1;
    *reinterpret_cast<u32x4*>(base + 16384 + B3_OFF(sr, sch)) = gb0;
  };
  const int wm = wid & 3, wn = wid >> 2;
  const int l15 = lane & 15, lg = lane >> 4, lq = (lane & 15) >> 2, lp = lane & 3;
  f32x4 acc[2][2];
#pragma unroll
  for (int i = 0; i < 2; ++i)
#pragma unroll
    for (int j = 0; j < 2; ++j) acc[i][j] = f32x4{0.f, 0.f, 0.f, 0.f};
  gload(0);
  swrite(0);
  __syncthreads();
  for (int t = 0; t < 16; ++t) {
    if (t < 15) gload(t + 1);
    const char* base = smem + (t & 1) * K3_STAGE;
#pragma unroll
    for (int ks = 0; ks < 2; ++ks) {
      bf16x8 a[2], b[2];
#pragma unroll
      for (int mi = 0; mi < 2; ++mi) a[mi] = *reinterpret_cast<const bf16x8*>(base + A_OFF(wm * 32 + mi * 16 + l15, ks * 4 + lg));
#pragma unroll
      for (int ni = 0; ni < 2; ++ni) {
        const int ch = wn * 4 + ni * 2 + (lp >> 1);
        const int kr = ks * 32 + lg * 8 + lq;
        const s16x4 lo = __builtin_amdgcn_ds_read_tr16_b64_v4i16((lds_s16x4*)(base + 16384 + B3_OFF(kr, ch) + 8 * (lp & 1)));
        const s16x4 hi = __builtin_amdgcn_ds_read_tr16_b64_v4i16((lds_s16x4*)(base + 16384 + B3_OFF(kr + 4, ch) + 8 * (lp & 1)));
        b[ni] = __builtin_shufflevector(lo, hi, 0, 1, 2, 3, 4, 5, 6, 7);
      }
#pragma unroll
      for (int mi = 0; mi < 2; ++mi)
#pragma unroll
        for (int ni = 0; ni < 2; ++ni)
          acc[mi][ni] = __builtin_amdgcn_mfma_f32_16x16x32_bf16(b[ni], a[mi], acc[mi][ni], 0, 0, 0);
    }
    if (t < 15) swrite((t + 1) & 1);
    __syncthreads();
  }
  const float* mf = reinterpret_cast<const float*>(ws + WS_MODF);
  const float* wr1 = reinterpret_cast<const float*>(ws + WS_WR1);
  u16* x1 = reinterpret_cast<u16*>(ws + WS_X1);
  float* rs = reinterpret_cast<float*>(ws + WS_RS2);
#pragma unroll
  for (int mi = 0; mi < 2; ++mi) {
    const int m = row0 + wm * 32 + mi * 16 + l15;
    float s1 = 0.f, s2 = 0.f, s3 = 0.f;
#pragma unroll
    for (int ni = 0; ni < 2; ++ni) {
      const int n = col0 + wn * 32 + ni * 16 + lg * 4;
      const u32x2 xv = *reinterpret_cast<const u32x2*>(x + (size_t)m * D_ + n);
      const f32x4 g = *reinterpret_cast<const f32x4*>(mf + 2048 + n);
      const f32x4 w = *reinterpret_cast<const f32x4*>(wr1 + n);
      float v[4];
      v[0] = rbf(lo2f(xv[0]) + rbf(g[0] * rbf(acc[mi][ni][0])));
      v[1] = rbf(hi2f(xv[0]) + rbf(g[1] * rbf(acc[mi][ni][1])));
      v[2] = rbf(lo2f(xv[1]) + rbf(g[2] * rbf(acc[mi][ni][2])));
      v[3] = rbf(hi2f(xv[1]) + rbf(g[3] * rbf(acc[mi][ni][3])));
      u32x2 ov = {pk2(v[0], v[1]), pk2(v[2], v[3])};
      *reinterpret_cast<u32x2*>(x1 + (size_t)m * D_ + n) = ov;
#pragma unroll
      for (int j = 0; j < 4; ++j) { s1 += v[j]; s2 += v[j] * v[j]; s3 += v[j] * w[j]; }
    }
    s1 += __shfl_xor(s1, 16); s1 += __shfl_xor(s1, 32);
    s2 += __shfl_xor(s2, 16); s2 += __shfl_xor(s2, 32);
    s3 += __shfl_xor(s3, 16); s3 += __shfl_xor(s3, 32);
    if (lg == 0) { atomicAdd(rs + m, s1); atomicAdd(rs + 2048 + m, s2); atomicAdd(rs + 4096 + m, s3); }
  }
}

__device__ __forceinline__ void row_stats2(const float* rs, int row, float& m, float& rstd) {
  const float mean = rs[row] * (1.f / D_);
  float var = rs[2048 + row] * (1.f / D_) - mean * mean;
  var = fmaxf(var, 0.f);
  m = rbf(mean);
  rstd = 1.f / rbf(sqrtf(rbf(rbf(var) + 1e-5f)));
}

__global__ __launch_bounds__(256) void k_gateup(const u16* __restrict__ wg, const u16* __restrict__ wu,
                                                char* __restrict__ ws, u16* __restrict__ out) {
  const int tid = threadIdx.x, lane = tid & 63, wid = tid >> 6;
  const float* rs = reinterpret_cast<const float*>(ws + WS_RS2);
  const float* mf = reinterpret_cast<const float*>(ws + WS_MODF);
  const u16* x1 = reinterpret_cast<const u16*>(ws + WS_X1);
  if (blockIdx.x < 224) {
    __shared__ float sv[4][2];
    __shared__ int si[4][2];
    __shared__ float hs[2][256];
    __shared__ float red[4][4][64];
    const int cg = blockIdx.x % 56, ks = blockIdx.x / 56;
    const int c8 = tid & 7, kr = tid >> 3;
    u32x4 g[8], u[8];
#pragma unroll
    for (int i = 0; i < 8; ++i) {
      const size_t off = (size_t)(ks * 256 + i * 32 + kr) * F_ + cg * 64 + c8 * 8;
      g[i] = *reinterpret_cast<const u32x4*>(wg + off);
      u[i] = *reinterpret_cast<const u32x4*>(wu + off);
    }
    const float W1 = reinterpret_cast<const float*>(ws + WS_W12)[0], W2 = reinterpret_cast<const float*>(ws + WS_W12)[1];
    float v1 = -3.0e38f, v2 = -3.0e38f; int i1 = 0x7fffffff, i2 = 0x7fffffff;
#pragma unroll
    for (int i = 0; i < 8; ++i) {
      const int row = i * 256 + tid;
      float m, rstd; row_stats2(rs, row, m, rstd);
      const float r = rbf(rstd * (rs[4096 + row] - m * W1) + W2);
      if (r > v1 || (r == v1 && row < i1)) { v2 = v1; i2 = i1; v1 = r; i1 = row; }
      else if (r > v2 || (r == v2 && row < i2)) { v2 = r; i2 = row; }
    }
#pragma unroll
    for (int o = 32; o > 0; o >>= 1) {
      const float a1 = __shfl_xor(v1, o), a2 = __shfl_xor(v2, o);
      const int b1 = __shfl_xor(i1, o), b2 = __shfl_xor(i2, o);
      if (a1 > v1 || (a1 == v1 && b1 < i1)) {
        if (v1 > a2 || (v1 == a2 && i1 < b2)) { v2 = v1; i2 = i1; } else { v2 = a2; i2 = b2; }
        v1 = a1; i1 = b1;
      } else if (a1 > v2 || (a1 == v2 && b1 < i2)) { v2 = a1; i2 = b1; }
    }
    if (lane == 0) { sv[wid][0] = v1; sv[wid][1] = v2; si[wid][0] = i1; si[wid][1] = i2; }
    __syncthreads();
    v1 = sv[0][0]; v2 = sv[0][1]; i1 = si[0][0]; i2 = si[0][1];
#pragma unroll
    for (int w = 1; w < 4; ++w) {
      const float a1 = sv[w][0], a2 = sv[w][1]; const int b1 = si[w][0], b2 = si[w][1];
      if (a1 > v1 || (a1 == v1 && b1 < i1)) {
        if (v1 > a2 || (v1 == a2 && i1 < b2)) { v2 = v1; i2 = i1; } else { v2 = a2; i2 = b2; }
        v1 = a1; i1 = b1;
      } else if (a1 > v2 || (a1 == v2 && b1 < i2)) { v2 = a1; i2 = b1; }
    }
    if (blockIdx.x == 0 && tid == 0) {
      int* sel = reinterpret_cast<int*>(ws + WS_SEL);
      sel[0] = i1; sel[1] = i2;
      reinterpret_cast<float*>(sel)[2] = v1; reinterpret_cast<float*>(sel)[3] = v2;
    }
    {
      const int k = ks * 256 + tid;
      const float a = rbf(1.f + mf[4096 + k]), s = mf[3072 + k];
      float m, rstd;
      row_stats2(rs, i1, m, rstd);
      hs[0][tid] = rbf((bf2f(x1[(size_t)i1 * D_ + k]) - m) * rstd * a + s);
      row_stats2(rs, i2, m, rstd);
      hs[1][tid] = rbf((bf2f(x1[(size_t)i2 * D_ + k]) - m) * rstd * a + s);
    }
    __syncthreads();
    float ag[2][8], au[2][8];
#pragma unroll
    for (int r = 0; r < 2; ++r)
#pragma unroll
      for (int j = 0; j < 8; ++j) { ag[r][j] = 0.f; au[r][j] = 0.f; }
#pragma unroll
    for (int i = 0; i < 8; ++i) {
      const float h0 = hs[0][i * 32 + kr], h1 = hs[1][i * 32 + kr];
#pragma unroll
      for (int j = 0; j < 4; ++j) {
        ag[0][2 * j] += h0 * lo2f(g[i][j]); ag[0][2 * j + 1] += h0 * hi2f(g[i][j]);
        ag[1][2 * j] += h1 * lo2f(g[i][j]); ag[1][2 * j + 1] += h1 * hi2f(g[i][j]);
        au[0][2 * j] += h0 * lo2f(u[i][j]); au[0][2 * j + 1] += h0 * hi2f(u[i][j]);
        au[1][2 * j] += h1 * lo2f(u[i][j]); au[1][2 * j + 1] += h1 * hi2f(u[i][j]);
      }
    }
#pragma unroll
    for (int r = 0; r < 2; ++r)
#pragma unroll
      for (int j = 0; j < 8; ++j) {
        float a = ag[r][j], b = au[r][j];
        a += __shfl_xor(a, 8); a += __shfl_xor(a, 16); a += __shfl_xor(a, 32);
        b += __shfl_xor(b, 8); b += __shfl_xor(b, 16); b += __shfl_xor(b, 32);
        if (lane < 8) { red[wid][r][lane * 8 + j] = a; red[wid][2 + r][lane * 8 + j] = b; }
      }
    __syncthreads();
    {
      const int q = tid >> 6, cc = tid & 63;
      const float v = red[0][q][cc] + red[1][q][cc] + red[2][q][cc] + red[3][q][cc];
      reinterpret_cast<float*>(ws + WS_GUP)[(size_t)(ks * 4 + q) * F_ + cg * 64 + cc] = v;
    }
  } else {
    const int rbase = (blockIdx.x - 224) * 8;
#pragma unroll 1
    for (int rr = wid; rr < 8; rr += 4) {
      const int row = rbase + rr;
      float m, rstd; row_stats2(rs, row, m, rstd);
#pragma unroll
      for (int half = 0; half < 2; ++half) {
        const int k = half * 512 + lane * 8;
        const u32x4 xv = *reinterpret_cast<const u32x4*>(x1 + (size_t)row * D_ + k);
        float a[8], s[8], gb[8];
        *reinterpret_cast<f32x4*>(a) = *reinterpret_cast<const f32x4*>(mf + 4096 + k);
        *reinterpret_cast<f32x4*>(a + 4) = *reinterpret_cast<const f32x4*>(mf + 4096 + k + 4);
        *reinterpret_cast<f32x4*>(s) = *reinterpret_cast<const f32x4*>(mf + 3072 + k);
        *reinterpret_cast<f32x4*>(s + 4) = *reinterpret_cast<const f32x4*>(mf + 3072 + k + 4);
        *reinterpret_cast<f32x4*>(gb) = *reinterpret_cast<const f32x4*>(mf + 5120 + k);
        *reinterpret_cast<f32x4*>(gb + 4) = *reinterpret_cast<const f32x4*>(mf + 5120 + k + 4);
        u32x4 ov;
#pragma unroll
        for (int j = 0; j < 4; ++j) {
          const float xa = lo2f(xv[j]), xb = hi2f(xv[j]);
          const float ha = rbf((xa - m) * rstd * rbf(1.f + a[2 * j]) + s[2 * j]);
          const float hb = rbf((xb - m) * rstd * rbf(1.f + a[2 * j + 1]) + s[2 * j + 1]);
          ov[j] = pk2(xa + rbf(gb[2 * j] * ha), xb + rbf(gb[2 * j + 1] * hb));
        }
        *reinterpret_cast<u32x4*>(out + (size_t)row * D_ + k) = ov;
      }
    }
  }
}

__global__ __launch_bounds__(256) void k_down(const u16* __restrict__ wd, char* __restrict__ ws, u16* __restrict__ out) {
  __shared__ float gs[2][224];
  __shared__ float red[4][2][64];
  __shared__ int last;
  const int tid = threadIdx.x, lane = tid & 63, wid = tid >> 6;
  const int cg = blockIdx.x & 15, ks = blockIdx.x >> 4;
  const int c8 = tid & 7, kr = tid >> 3;
  u32x4 w[7];
#pragma unroll
  for (int i = 0; i < 7; ++i)
    w[i] = *reinterpret_cast<const u32x4*>(wd + (size_t)(ks * 224 + i * 32 + kr) * D_ + cg * 64 + c8 * 8);
  const float* gup = reinterpret_cast<const float*>(ws + WS_GUP);
  if (tid < 224) {
    const int f = ks * 224 + tid;
#pragma unroll
    for (int r = 0; r < 2; ++r) {
      float g = 0.f, u = 0.f;
#pragma unroll
      for (int p = 0; p < 4; ++p) { g += gup[(size_t)(p * 4 + r) * F_ + f]; u += gup[(size_t)(p * 4 + 2 + r) * F_ + f]; }
      gs[r][tid] = rbf(rbf(silu_f(rbf(g))) * rbf(u));
    }
  }
  __syncthreads();
  float a0[8], a1[8];
#pragma unroll
  for (int j = 0; j < 8; ++j) { a0[j] = 0.f; a1[j] = 0.f; }
#pragma unroll
  for (int i = 0; i < 7; ++i) {
    const float g0 = gs[0][i * 32 + kr], g1 = gs[1][i * 32 + kr];
#pragma unroll
    for (int j = 0; j < 4; ++j) {
      a0[2 * j] += g0 * lo2f(w[i][j]); a0[2 * j + 1] += g0 * hi2f(w[i][j]);
      a1[2 * j] += g1 * lo2f(w[i][j]); a1[2 * j + 1] += g1 * hi2f(w[i][j]);
    }
  }
#pragma unroll
  for (int j = 0; j < 8; ++j) {
    float a = a0[j], b = a1[j];
    a += __shfl_xor(a, 8); a += __shfl_xor(a, 16); a += __shfl_xor(a, 32);
    b += __shfl_xor(b, 8); b += __shfl_xor(b, 16); b += __shfl_xor(b, 32);
    if (lane < 8) { red[wid][0][lane * 8 + j] = a; red[wid][1][lane * 8 + j] = b; }
  }
  __syncthreads();
  float* yp = reinterpret_cast<float*>(ws + WS_YP);
  if (tid < 128) {
    const int r = tid >> 6, cc = tid & 63;
    yp[(size_t)(ks * 2 + r) * D_ + cg * 64 + cc] = red[0][r][cc] + red[1][r][cc] + red[2][r][cc] + red[3][r][cc];
  }
  __threadfence();
  __syncthreads();
  if (tid == 0) {
    const unsigned t = atomicAdd(reinterpret_cast<unsigned*>(ws + WS_CNT) + cg, 1u);
    last = (t == 15u);
  }
  __syncthreads();
  if (last) {
    __threadfence();
    if (tid < 128) {
      const int r = tid >> 6, cc = tid & 63, n = cg * 64 + cc;
      float y = 0.f;
#pragma unroll
      for (int p = 0; p < 16; ++p) y += __builtin_nontemporal_load(yp + (size_t)(p * 2 + r) * D_ + n);
      const int* sel = reinterpret_cast<const int*>(ws + WS_SEL);
      const int row = sel[r];
      const float rv = reinterpret_cast<const float*>(sel)[2 + r];
      const float sg = rbf(1.f / (1.f + __expf(-rv)));
      y = rbf(rbf(y) * sg);
      const float* rs = reinterpret_cast<const float*>(ws + WS_RS2);
      const float* mf = reinterpret_cast<const float*>(ws + WS_MODF);
      const u16* x1 = reinterpret_cast<const u16*>(ws + WS_X1);
      float m, rstd; row_stats2(rs, row, m, rstd);
      const float xv = bf2f(x1[(size_t)row * D_ + n]);
      const float h2 = rbf((xv - m) * rstd * rbf(1.f + mf[4096 + n]) + mf[3072 + n]);
      const float B = rbf(h2 + y);
      out[(size_t)row * D_ + n] = (u16)(pk2(xv + rbf(mf[5120 + n] * B), 0.f) & 0xffff);
    }
  }
}

extern "C" void kernel_launch(void* const* d_in, const int* in_sizes, int n_in, void* d_out, int out_size,
                              void* d_ws, size_t ws_size, hipStream_t stream) {
  const u16* x = (const u16*)d_in[0];
  const u16* wqkv = (const u16*)d_in[1];
  const u16* wphi = (const u16*)d_in[2];
  const u16* wo = (const u16*)d_in[3];
  const u16* wr = (const u16*)d_in[5];
  const u16* wg = (const u16*)d_in[6];
  const u16* wu = (const u16*)d_in[7];
  const u16* wd = (const u16*)d_in[8];
  const u16* c = (const u16*)d_in[9];
  const u16* wmod = (const u16*)d_in[10];
  char* ws = (char*)d_ws;
  u16* out = (u16*)d_out;
  hipFuncSetAttribute((const void*)k_qkv, hipFuncAttributeMaxDynamicSharedMemorySize, K1_LDS);
  k_prep<<<896, 256, 0, stream>>>(x, c, wmod, ws);
  k_qkv<<<256, 512, K1_LDS, stream>>>(x, wqkv, wphi, ws);
  k_o<<<256, 256, 0, stream>>>(wphi, wr, ws);
  k_wo<<<256, 512, 0, stream>>>(x, wo, ws);
  k_gateup<<<480, 256, 0, stream>>>(wg, wu, ws, out);
  k_down<<<256, 256, 0, stream>>>(wd, ws, out);
}
```

```cpp
#include <hip/hip_runtime.h>
#include <stdint.h>

typedef unsigned short u16;
typedef short bf16x8 __attribute__((ext_vector_type(8)));
typedef short s16x4 __attribute__((ext_vector_type(4)));
typedef float f32x2 __attribute__((ext_vector_type(2)));
typedef float f32x4 __attribute__((ext_vector_type(4)));
typedef float f32x16 __attribute__((ext_vector_type(16)));
typedef unsigned u32x4 __attribute__((ext_vector_type(4)));
typedef unsigned u32x2 __attribute__((ext_vector_type(2)));
typedef __bf16 bf16x2v __attribute__((ext_vector_type(2)));
typedef __attribute__((address_space(3))) s16x4 lds_s16x4;

#define S_ 2048
#define D_ 1024
#define H_ 32
#define F_ 3584

#define WS_MODP   0u
#define WS_STATS  (WS_MODP + 4u*6144u*4u)
#define WS_RS2    (WS_STATS + 2048u*8u)
#define WS_CNT    (WS_RS2 + 3u*2048u*4u)
#define WS_MODF   (WS_CNT + 256u)
#define WS_WR1    (WS_MODF + 6144u*4u)
#define WS_W12    (WS_WR1 + 1024u*4u)
#define WS_SEL    (WS_W12 + 64u)
#define WS_GUP    (WS_SEL + 64u)
#define WS_YP     (WS_GUP + 4u*2u*2u*3584u*4u)
#define WS_Q      (WS_YP + 2u*1024u*4u)
#define WS_O      (WS_Q + 2048u*1024u*2u)
#define WS_X1     (WS_O + 2048u*1024u*2u)
#define WS_KVP    (WS_X1 + 2048u*1024u*2u)
#define WS_END    (WS_KVP + 16u*32u*2112u*4u)

__device__ __forceinline__ float bf2f(u16 u) { return __uint_as_float(((unsigned)u) << 16); }
__device__ __forceinline__ unsigned pk2(float a, float b) {
  f32x2 f = {a, b}; bf16x2v h = __builtin_convertvector(f, bf16x2v); return __builtin_bit_cast(unsigned, h);
}
__device__ __forceinline__ float rbf(float a) {
  return __uint_as_float(pk2(a, 0.f) << 16);
}
__device__ __forceinline__ float lo2f(unsigned w) { return __uint_as_float(w << 16); }
__device__ __forceinline__ float hi2f(unsigned w) { return __uint_as_float(w & 0xffff0000u); }
__device__ __forceinline__ float wave_sum(float v) {
  for (int o = 32; o > 0; o >>= 1) v += __shfl_xor(v, o);
  return v;
}
__device__ __forceinline__ float silu_f(float x) { return x / (1.f + __expf(-x)); }

__global__ __launch_bounds__(256) void k_prep(const u16* __restrict__ x, const u16* __restrict__ c,
                                              const u16* __restrict__ wmod, char* __restrict__ ws) {
  const int tid = threadIdx.x, lane = tid & 63, wid = tid >> 6;
  if (blockIdx.x < 384) {
    __shared__ float red[4][64];
    const int cg = blockIdx.x % 96, ks = blockIdx.x / 96;
    const int c8 = tid & 7, kr = tid >> 3;
    u32x4 w[8];
    float sc[8];
#pragma unroll
    for (int i = 0; i < 8; ++i) {
      const int k = ks * 256 + i * 32 + kr;
      w[i] = *reinterpret_cast<const u32x4*>(wmod + (size_t)k * 6144 + cg * 64 + c8 * 8);
      sc[i] = rbf(silu_f(bf2f(c[k])));
    }
    float acc[8] = {0, 0, 0, 0, 0, 0, 0, 0};
#pragma unroll
    for (int i = 0; i < 8; ++i)
#pragma unroll
      for (int j = 0; j < 4; ++j) { acc[2 * j] += sc[i] * lo2f(w[i][j]); acc[2 * j + 1] += sc[i] * hi2f(w[i][j]); }
#pragma unroll
    for (int j = 0; j < 8; ++j) {
      float v = acc[j];
      v += __shfl_xor(v, 8); v += __shfl_xor(v, 16); v += __shfl_xor(v, 32);
      acc[j] = v;
    }
    if (lane < 8)
#pragma unroll
      for (int j = 0; j < 8; ++j) red[wid][lane * 8 + j] = acc[j];
    __syncthreads();
    if (tid < 64) {
      float v = red[0][tid] + red[1][tid] + red[2][tid] + red[3][tid];
      reinterpret_cast<float*>(ws + WS_MODP)[ks * 6144 + cg * 64 + tid] = v;
    }
  } else {
    const int row = (blockIdx.x - 384) * 4 + wid;
    const u32x4 a = *reinterpret_cast<const u32x4*>(x + (size_t)row * D_ + lane * 8);
    const u32x4 b = *reinterpret_cast<const u32x4*>(x + (size_t)row * D_ + 512 + lane * 8);
    float s = 0.f;
#pragma unroll
    for (int j = 0; j < 4; ++j) s += lo2f(a[j]) + hi2f(a[j]) + lo2f(b[j]) + hi2f(b[j]);
    const float mean = wave_sum(s) * (1.f / D_);
    float q = 0.f;
#pragma unroll
    for (int j = 0; j < 4; ++j) {
      float d;
      d = lo2f(a[j]) - mean; q += d * d; d = hi2f(a[j]) - mean; q += d * d;
      d = lo2f(b[j]) - mean; q += d * d; d = hi2f(b[j]) - mean; q += d * d;
    }
    const float var = wave_sum(q) * (1.f / D_);
    if (lane == 0) {
      const float sd = rbf(sqrtf(rbf(rbf(var) + 1e-5f)));
      float* st = reinterpret_cast<float*>(ws + WS_STATS);
      st[row * 2] = rbf(mean);
      st[row * 2 + 1] = 1.f / sd;
      float* rs = reinterpret_cast<float*>(ws + WS_RS2);
      rs[row] = 0.f; rs[2048 + row] = 0.f; rs[4096 + row] = 0.f;
    }
    if (blockIdx.x == 384 && tid < 64) reinterpret_cast<unsigned*>(ws + WS_CNT)[tid] = 0u;
    {
      const int bi = blockIdx.x - 384;
      if (bi < 2) reinterpret_cast<f32x4*>(ws + WS_YP)[bi * 256 + tid] = f32x4{0.f, 0.f, 0.f, 0.f};
    }
  }
}

#define A_OFF(row, ch) ((row) * 128 + ((((ch) ^ ((row) & 7))) << 4))
#define B_OFF(row, ch) ((((row) >> 3) * 6 + ((ch) >> 2)) * 512 + ((row) & 7) * 64 + ((((ch) & 3) ^ (((row) >> 2) & 3)) << 4))
#define K1_STAGE 40960
#define K1_MOD (2 * K1_STAGE)
#define K1_WPHI (K1_MOD + 8192)
#define K1_LDS (K1_WPHI + 4096)
#define C_STRIDE 400
#define PHI_OFF (128 * C_STRIDE)
#define PHI_STRIDE 144

__global__ __launch_bounds__(512) void k_qkv(const u16* __restrict__ x, const u16* __restrict__ wqkv,
                                             const u16* __restrict__ wphi, char* __restrict__ ws) {
  extern __shared__ __attribute__((aligned(16))) char smem[];
  const int tid = threadIdx.x, lane = tid & 63, wid = tid >> 6;
  const int xcd = blockIdx.x & 7, idx = blockIdx.x >> 3;
  const int mt = (xcd & 1) * 8 + (idx & 7), hp = (xcd >> 1) * 4 + (idx >> 3);
  const int row0 = mt * 128;
  float* lmod = reinterpret_cast<float*>(smem + K1_MOD);
  const int sr = tid >> 3, sch = tid & 7;
  const float* st = reinterpret_cast<const float*>(ws + WS_STATS);
  const float m0 = st[(row0 + sr) * 2], r0 = st[(row0 + sr) * 2 + 1];
  const float m1 = st[(row0 + sr + 64) * 2], r1 = st[(row0 + sr + 64) * 2 + 1];
  const u16* xa0 = x + (size_t)(row0 + sr) * D_ + sch * 8;
  const u16* xa1 = xa0 + 64 * D_;
  const u16* wb = wqkv + (size_t)sr * 3072 + hp * 64 + sch * 8;

  u32x4 rg[4][5];
  auto gload = [&](int t) {
    u32x4* g = rg[t & 3];
    g[0] = *reinterpret_cast<const u32x4*>(xa0 + t * 64);
    g[1] = *reinterpret_cast<const u32x4*>(xa1 + t * 64);
    const u16* p = wb + (size_t)t * 64 * 3072;
    g[2] = *reinterpret_cast<const u32x4*>(p);
    g[3] = *reinterpret_cast<const u32x4*>(p + 1024);
    g[4] = *reinterpret_cast<const u32x4*>(p + 2048);
  };
  auto swrite = [&](int t, int buf) {
    char* base = smem + buf * K1_STAGE;
    const u32x4 ga0 = rg[t & 3][0], ga1 = rg[t & 3][1], gb0 = rg[t & 3][2], gb1 = rg[t & 3][3], gb2 = rg[t & 3][4];
    const f32x4 a0 = *reinterpret_cast<const f32x4*>(lmod + t * 64 + sch * 8);
    const f32x4 a1 = *reinterpret_cast<const f32x4*>(lmod + t * 64 + sch * 8 + 4);
    const f32x4 s0 = *reinterpret_cast<const f32x4*>(lmod + 1024 + t * 64 + sch * 8);
    const f32x4 s1 = *reinterpret_cast<const f32x4*>(lmod + 1024 + t * 64 + sch * 8 + 4);
    const float av[8] = {a0[0], a0[1], a0[2], a0[3], a1[0], a1[1], a1[2], a1[3]};
    const float sv[8] = {s0[0], s0[1], s0[2], s0[3], s1[0], s1[1], s1[2], s1[3]};
    u32x4 h0, h1;
#pragma unroll
    for (int j = 0; j < 4; ++j) {
      h0[j] = pk2((lo2f(ga0[j]) - m0) * r0 * av[2 * j] + sv[2 * j], (hi2f(ga0[j]) - m0) * r0 * av[2 * j + 1] + sv[2 * j + 1]);
      h1[j] = pk2((lo2f(ga1[j]) - m1) * r1 * av[2 * j] + sv[2 * j], (hi2f(ga1[j]) - m1) * r1 * av[2 * j + 1] + sv[2 * j + 1]);
    }
    *reinterpret_cast<u32x4*>(base + A_OFF(sr, sch)) = h0;
    *reinterpret_cast<u32x4*>(base + A_OFF(sr + 64, sch)) = h1;
    *reinterpret_cast<u32x4*>(base + 16384 + B_OFF(sr, sch)) = gb0;
    *reinterpret_cast<u32x4*>(base + 16384 + B_OFF(sr, 8 + sch)) = gb1;
    *reinterpret_cast<u32x4*>(base + 16384 + B_OFF(sr, 16 + sch)) = gb2;
  };

  const int wm = wid & 1, wn = wid >> 1;
  const int l15 = lane & 15, lg = lane >> 4, lq = (lane & 15) >> 2, lp = lane & 3;
  f32x4 acc[4][3];
#pragma unroll
  for (int i = 0; i < 4; ++i)
#pragma unroll
    for (int j = 0; j < 3; ++j) acc[i][j] = f32x4{0.f, 0.f, 0.f, 0.f};

  gload(0); gload(1); gload(2);
  {
    if (tid < 256) *reinterpret_cast<u32x4*>(smem + K1_WPHI + tid * 16) = *reinterpret_cast<const u32x4*>(wphi + (size_t)hp * 2048 + tid * 8);
    const float* mp = reinterpret_cast<const float*>(ws + WS_MODP);
#pragma unroll
    for (int i = tid; i < 1024; i += 512) {
      const float sa = mp[i] + mp[6144 + i] + mp[2 * 6144 + i] + mp[3 * 6144 + i];
      const float ca = mp[1024 + i] + mp[6144 + 1024 + i] + mp[2 * 6144 + 1024 + i] + mp[3 * 6144 + 1024 + i];
      lmod[i] = rbf(1.f + rbf(ca));
      lmod[1024 + i] = rbf(sa);
    }
  }
  __syncthreads();
  swrite(0, 0);
  __syncthreads();
#pragma unroll
  for (int t = 0; t < 16; ++t) {
    if (t < 13) gload(t + 3);
    const char* base = smem + (t & 1) * K1_STAGE;
#pragma unroll
    for (int ks = 0; ks < 2; ++ks) {
      bf16x8 a[4], b[3];
#pragma unroll
      for (int mi = 0; mi < 4; ++mi) {
        const int r = wm * 64 + mi * 16 + l15;
        a[mi] = *reinterpret_cast<const bf16x8*>(base + A_OFF(r, ks * 4 + lg));
      }
#pragma unroll
      for (int ni = 0; ni < 3; ++ni) {
        const int ch = wn * 6 + ni * 2 + (lp >> 1);
        const int kr = ks * 32 + lg * 8 + lq;
        const s16x4 lo = __builtin_amdgcn_ds_read_tr16_b64_v4i16((lds_s16x4*)(base + 16384 + B_OFF(kr, ch) + 8 * (lp & 1)));
        const s16x4 hi = __builtin_amdgcn_ds_read_tr16_b64_v4i16((lds_s16x4*)(base + 16384 + B_OFF(kr + 4, ch) + 8 * (lp & 1)));
        b[ni] = __builtin_shufflevector(lo, hi, 0, 1, 2, 3, 4, 5, 6, 7);
      }
#pragma unroll
      for (int mi = 0; mi < 4; ++mi)
#pragma unroll
        for (int ni = 0; ni < 3; ++ni)
          acc[mi][ni] = __builtin_amdgcn_mfma_f32_16x16x32_bf16(b[ni], a[mi], acc[mi][ni], 0, 0, 0);
    }
    if (t < 15) swrite(t + 1, (t + 1) & 1);
    __syncthreads();
  }
#pragma unroll
  for (int mi = 0; mi < 4; ++mi)
#pragma unroll
    for (int ni = 0; ni < 3; ++ni) {
      const int m = wm * 64 + mi * 16 + l15, n = wn * 48 + ni * 16 + lg * 4;
      u32x2 v = {pk2(acc[mi][ni][0], acc[mi][ni][1]), pk2(acc[mi][ni][2], acc[mi][ni][3])};
      *reinterpret_cast<u32x2*>(smem + m * C_STRIDE + n * 2) = v;
    }
  __syncthreads();
  {
    u16* q = reinterpret_cast<u16*>(ws + WS_Q);
#pragma unroll
    for (int i = 0; i < 2; ++i) {
      const int r = sr + i * 64;
      const u32x4 v = *reinterpret_cast<const u32x4*>(smem + r * C_STRIDE + sch * 16);
      *reinterpret_cast<u32x4*>(q + (size_t)(row0 + r) * D_ + hp * 64 + sch * 8) = v;
    }
  }
  const int l31 = lane & 31, lh = lane >> 5;
  {
    const int hh = wid & 1, rt = wid >> 1;
    const u16* wp = reinterpret_cast<const u16*>(smem + K1_WPHI) + hh * 1024;
    f32x16 xp;
#pragma unroll
    for (int i = 0; i < 16; ++i) xp[i] = 0.f;
#pragma unroll
    for (int s = 0; s < 2; ++s) {
      bf16x8 a;
#pragma unroll
      for (int j = 0; j < 8; ++j) a[j] = (short)wp[(16 * s + 8 * lh + j) * 32 + l31];
      const bf16x8 b = *reinterpret_cast<const bf16x8*>(smem + (rt * 32 + l31) * C_STRIDE + (64 + hh * 32 + 16 * s + 8 * lh) * 2);
      xp = __builtin_amdgcn_mfma_f32_32x32x16_bf16(a, b, xp, 0, 0, 0);
    }
    float mx = -3.0e38f, mn = 3.0e38f;
#pragma unroll
    for (int i = 0; i < 16; ++i) { xp[i] = rbf(xp[i]); mx = fmaxf(mx, xp[i]); mn = fminf(mn, xp[i]); }
    mx = fmaxf(mx, __shfl_xor(mx, 32)); mn = fminf(mn, __shfl_xor(mn, 32));
    float ep[16], en[16], sp = 0.f, sn = 0.f;
#pragma unroll
    for (int i = 0; i < 16; ++i) { ep[i] = __expf(xp[i] - mx); sp += ep[i]; en[i] = __expf(mn - xp[i]); sn += en[i]; }
    sp += __shfl_xor(sp, 32); sn += __shfl_xor(sn, 32);
    const float ip = 1.f / sp, in_ = 1.f / sn;
    char* ph = smem + PHI_OFF + (hh * 128 + rt * 32 + l31) * PHI_STRIDE;
#pragma unroll
    for (int g = 0; g < 4; ++g) {
      u32x2 v = {pk2(ep[4 * g] * ip, ep[4 * g + 1] * ip), pk2(ep[4 * g + 2] * ip, ep[4 * g + 3] * ip)};
      *reinterpret_cast<u32x2*>(ph + (8 * g + 4 * lh) * 2) = v;
      u32x2 w = {pk2(en[4 * g] * in_, en[4 * g + 1] * in_), pk2(en[4 * g + 2] * in_, en[4 * g + 3] * in_)};
      *reinterpret_cast<u32x2*>(ph + 64 + (8 * g + 4 * lh) * 2) = w;
    }
  }
  __syncthreads();
  if (wid < 4) {
    const int hh = wid & 1, ft = wid >> 1;
    const int blk = (lane >> 4) & 1;
    f32x16 kv, zz;
#pragma unroll
    for (int i = 0; i < 16; ++i) { kv[i] = 0.f; zz[i] = 0.f; }
    bf16x8 ones;
#pragma unroll
    for (int j = 0; j < 8; ++j) ones[j] = (short)0x3f80;
#pragma unroll
    for (int s = 0; s < 8; ++s) {
      const char* pa = smem + PHI_OFF + (hh * 128 + 16 * s + 8 * lh + lq) * PHI_STRIDE + (ft * 32 + 16 * blk + 4 * lp) * 2;
      const s16x4 alo = __builtin_amdgcn_ds_read_tr16_b64_v4i16((lds_s16x4*)pa);
      const s16x4 ahi = __builtin_amdgcn_ds_read_tr16_b64_v4i16((lds_s16x4*)(pa + 4 * PHI_STRIDE));
      const bf16x8 a = __builtin_shufflevector(alo, ahi, 0, 1, 2, 3, 4, 5, 6, 7);
      const char* pb = smem + (16 * s + 8 * lh + lq) * C_STRIDE + (128 + hh * 32 + 16 * blk + 4 * lp) * 2;
      const s16x4 blo = __builtin_amdgcn_ds_read_tr16_b64_v4i16((lds_s16x4*)pb);
      const s16x4 bhi = __builtin_amdgcn_ds_read_tr16_b64_v4i16((lds_s16x4*)(pb + 4 * C_STRIDE));
      const bf16x8 b = __builtin_shufflevector(blo, bhi, 0, 1, 2, 3, 4, 5, 6, 7);
      kv = __builtin_amdgcn_mfma_f32_32x32x16_bf16(a, b, kv, 0, 0, 0);
      zz = __builtin_amdgcn_mfma_f32_32x32x16_bf16(a, ones, zz, 0, 0, 0);
    }
    float* kp = reinterpret_cast<float*>(ws + WS_KVP) + (size_t)((hp * 2 + hh) * 16 + mt) * 2112;
#pragma unroll
    for (int i = 0; i < 16; ++i) {
      const int f = ft * 32 + (i & 3) + 8 * (i >> 2) + 4 * lh;
      kp[f * 32 + l31] = kv[i];
      if (l31 == 0) kp[2048 + f] = zz[i];
    }
  }
}

__global__ __launch_bounds__(256) void k_o(const u16* __restrict__ wphi, const u16* __restrict__ wr, char* __restrict__ ws) {
  __shared__ __attribute__((aligned(16))) u16 kvt[32 * 68 + 64];
  __shared__ float zs[64];
  __shared__ float red[8];
  const int tid = threadIdx.x, lane = tid & 63, wid = tid >> 6;
  if (blockIdx.x == 256) {
    const float* mp = reinterpret_cast<const float*>(ws + WS_MODP);
    float* mf = reinterpret_cast<float*>(ws + WS_MODF);
    float* wr1 = reinterpret_cast<float*>(ws + WS_WR1);
    float s1 = 0.f, s2 = 0.f;
#pragma unroll
    for (int i = tid; i < 1024; i += 256) {
#pragma unroll
      for (int q = 0; q < 6; ++q)
        mf[q * 1024 + i] = rbf(mp[q * 1024 + i] + mp[6144 + q * 1024 + i] + mp[2 * 6144 + q * 1024 + i] + mp[3 * 6144 + q * 1024 + i]);
      const float sb = rbf(mp[3072 + i] + mp[6144 + 3072 + i] + mp[2 * 6144 + 3072 + i] + mp[3 * 6144 + 3072 + i]);
      const float cb = rbf(mp[4096 + i] + mp[6144 + 4096 + i] + mp[2 * 6144 + 4096 + i] + mp[3 * 6144 + 4096 + i]);
      const float w = bf2f(wr[i]);
      const float a = rbf(1.f + cb) * w;
      wr1[i] = a; s1 += a; s2 += sb * w;
    }
    s1 = wave_sum(s1); s2 = wave_sum(s2);
    if (lane == 0) { red[wid] = s1; red[4 + wid] = s2; }
    __syncthreads();
    if (tid == 0) {
      float* w12 = reinterpret_cast<float*>(ws + WS_W12);
      w12[0] = red[0] + red[1] + red[2] + red[3];
      w12[1] = red[4] + red[5] + red[6] + red[7];
    }
    return;
  }
  const int h = blockIdx.x & 31, rc = blockIdx.x >> 5;
  const int l31 = lane & 31, lh = lane >> 5;
  const u16* q = reinterpret_cast<const u16*>(ws + WS_Q);
  bf16x8 qb[2][2];
#pragma unroll
  for (int rt = 0; rt < 2; ++rt)
#pragma unroll
    for (int s = 0; s < 2; ++s)
      qb[rt][s] = *reinterpret_cast<const bf16x8*>(q + (size_t)(rc * 256 + wid * 64 + rt * 32 + l31) * D_ + h * 32 + 16 * s + 8 * lh);
  const u16* wp = wphi + (size_t)h * 1024;
  bf16x8 wa[2];
#pragma unroll
  for (int s = 0; s < 2; ++s)
#pragma unroll
    for (int j = 0; j < 8; ++j) wa[s][j] = (short)wp[(16 * s + 8 * lh + j) * 32 + l31];
  {
    const f32x4* kp = reinterpret_cast<const f32x4*>(ws + WS_KVP) + (size_t)h * 16 * 528;
    f32x4 s0 = {0.f, 0.f, 0.f, 0.f}, s1 = s0, s2 = s0;
#pragma unroll
    for (int m = 0; m < 16; ++m) {
      s0 += kp[m * 528 + tid];
      s1 += kp[m * 528 + 256 + tid];
    }
    if (wid == 0) {
#pragma unroll
      for (int m = 0; m < 16; ++m) s2 += kp[m * 528 + 512 + (lane & 15)];
    }
    {
      const int f = tid >> 3, d = (tid & 7) * 4;
#pragma unroll
      for (int j = 0; j < 4; ++j) {
        kvt[(d + j) * 68 + f] = (u16)(pk2(s0[j], 0.f) & 0xffff);
        kvt[(d + j) * 68 + 32 + f] = (u16)(pk2(s1[j], 0.f) & 0xffff);
      }
    }
    if (tid < 16)
#pragma unroll
      for (int j = 0; j < 4; ++j) zs[tid * 4 + j] = rbf(s2[j]);
  }
  __syncthreads();
  bf16x8 ka[4];
#pragma unroll
  for (int s = 0; s < 4; ++s) {
    const s16x4 lo = *reinterpret_cast<const s16x4*>(kvt + l31 * 68 + (s >> 1) * 32 + 16 * (s & 1) + 4 * lh);
    const s16x4 hi = *reinterpret_cast<const s16x4*>(kvt + l31 * 68 + (s >> 1) * 32 + 16 * (s & 1) + 8 + 4 * lh);
    ka[s] = __builtin_shufflevector(lo, hi, 0, 1, 2, 3, 4, 5, 6, 7);
  }
  float zp[16], zn[16];
#pragma unroll
  for (int i = 0; i < 16; ++i) { const int f = (i & 3) + 8 * (i >> 2) + 4 * lh; zp[i] = zs[f]; zn[i] = zs[32 + f]; }
  u16* o = reinterpret_cast<u16*>(ws + WS_O);
#pragma unroll
  for (int rt = 0; rt < 2; ++rt) {
    const int row = rc * 256 + wid * 64 + rt * 32 + l31;
    f32x16 xp;
#pragma unroll
    for (int i = 0; i < 16; ++i) xp[i] = 0.f;
#pragma unroll
    for (int s = 0; s < 2; ++s) {
      xp = __builtin_amdgcn_mfma_f32_32x32x16_bf16(wa[s], qb[rt][s], xp, 0, 0, 0);
    }
    float mx = -3.0e38f, mn = 3.0e38f;
#pragma unroll
    for (int i = 0; i < 16; ++i) { xp[i] = rbf(xp[i]); mx = fmaxf(mx, xp[i]); mn = fminf(mn, xp[i]); }
    mx = fmaxf(mx, __shfl_xor(mx, 32)); mn = fminf(mn, __shfl_xor(mn, 32));
    float ep[16], en[16], sp = 0.f, sn = 0.f;
#pragma unroll
    for (int i = 0; i < 16; ++i) { ep[i] = __expf(xp[i] - mx); sp += ep[i]; en[i] = __expf(mn - xp[i]); sn += en[i]; }
    sp += __shfl_xor(sp, 32); sn += __shfl_xor(sn, 32);
    const float ip = 1.f / sp, in_ = 1.f / sn;
    float den = 0.f;
    bf16x8 pb[4];
#pragma unroll
    for (int s = 0; s < 2; ++s) {
      u32x4 wpk, wnk;
#pragma unroll
      for (int j = 0; j < 4; ++j) {
        wpk[j] = pk2(ep[8 * s + 2 * j] * ip, ep[8 * s + 2 * j + 1] * ip);
        wnk[j] = pk2(en[8 * s + 2 * j] * in_, en[8 * s + 2 * j + 1] * in_);
        den += lo2f(wpk[j]) * zp[8 * s + 2 * j] + hi2f(wpk[j]) * zp[8 * s + 2 * j + 1];
        den += lo2f(wnk[j]) * zn[8 * s + 2 * j] + hi2f(wnk[j]) * zn[8 * s + 2 * j + 1];
      }
      pb[s] = __builtin_bit_cast(bf16x8, wpk);
      pb[2 + s] = __builtin_bit_cast(bf16x8, wnk);
    }
    den += __shfl_xor(den, 32);
    den = rbf(rbf(den) + 1e-6f);
    f32x16 num;
#pragma unroll
    for (int i = 0; i < 16; ++i) num[i] = 0.f;
#pragma unroll
    for (int s = 0; s < 4; ++s) num = __builtin_amdgcn_mfma_f32_32x32x16_bf16(ka[s], pb[s], num, 0, 0, 0);
#pragma unroll
    for (int g = 0; g < 4; ++g) {
      u32x2 v = {pk2(rbf(num[4 * g]) / den, rbf(num[4 * g + 1]) / den), pk2(rbf(num[4 * g + 2]) / den, rbf(num[4 * g + 3]) / den)};
      *reinterpret_cast<u32x2*>(o + (size_t)row * D_ + h * 32 + 8 * g + 4 * lh) = v;
    }
  }
}

#define B3_OFF(row, ch) ((((row) >> 3) * 2 + ((ch) >> 2)) * 512 + ((row) & 7) * 64 + ((((ch) & 3) ^ (((row) >> 2) & 3)) << 4))
#define K3_STAGE 24576
__global__ __launch_bounds__(512) void k_wo(const u16* __restrict__ x, const u16* __restrict__ wo, char* __restrict__ ws) {
  __shared__ __attribute__((aligned(16))) char smem[2 * K3_STAGE];
  const int tid = threadIdx.x, lane = tid & 63, wid = tid >> 6;
  const int xcd = blockIdx.x & 7, idx = blockIdx.x >> 3;
  const int mt = (xcd & 3) * 4 + (idx & 3), nt = (xcd >> 2) * 8 + (idx >> 2);
  const int row0 = mt * 128, col0 = nt * 64;
  const u16* o = reinterpret_cast<const u16*>(ws + WS_O);
  const int sr = tid >> 3, sch = tid & 7;
  const u16* pa0 = o + (size_t)(row0 + sr) * D_ + sch * 8;
  const u16* pa1 = pa0 + 64 * D_;
  const u16* pb = wo + (size_t)sr * D_ + col0 + sch * 8;
  u32x4 rg[4][3];
  auto gload = [&](int t) {
    rg[t & 3][0] = *reinterpret_cast<const u32x4*>(pa0 + t * 64);
    rg[t & 3][1] = *reinterpret_cast<const u32x4*>(pa1 + t * 64);
    rg[t & 3][2] = *reinterpret_cast<const u32x4*>(pb + (size_t)t * 64 * D_);
  };
  auto swrite = [&](int t, int buf) {
    char* base = smem + buf * K3_STAGE;
    const u32x4 ga0 = rg[t & 3][0], ga1 = rg[t & 3][1], gb0 = rg[t & 3][2];
    *reinterpret_cast<u32x4*>(base + A_OFF(sr, sch)) = ga0;
    *reinterpret_cast<u32x4*>(base + A_OFF(sr + 64, sch)) = ga1;
    *reinterpret_cast<u32x4*>(base + 16384 + B3_OFF(sr, sch)) = gb0;
  };
  const int wm = wid & 3, wn = wid >> 2;
  const int l15 = lane & 15, lg = lane >> 4, lq = (lane & 15) >> 2, lp = lane & 3;
  f32x4 acc[2][2];
#pragma unroll
  for (int i = 0; i < 2; ++i)
#pragma unroll
    for (int j = 0; j < 2; ++j) acc[i][j] = f32x4{0.f, 0.f, 0.f, 0.f};
  gload(0); gload(1); gload(2);
  swrite(0, 0);
  __syncthreads();
#pragma unroll
  for (int t = 0; t < 16; ++t) {
    if (t < 13) gload(t + 3);
    const char* base = smem + (t & 1) * K3_STAGE;
#pragma unroll
    for (int ks = 0; ks < 2; ++ks) {
      bf16x8 a[2], b[2];
#pragma unroll
      for (int mi = 0; mi < 2; ++mi) a[mi] = *reinterpret_cast<const bf16x8*>(base + A_OFF(wm * 32 + mi * 16 + l15, ks * 4 + lg));
#pragma unroll
      for (int ni = 0; ni < 2; ++ni) {
        const int ch = wn * 4 + ni * 2 + (lp >> 1);
        const int kr = ks * 32 + lg * 8 + lq;
        const s16x4 lo = __builtin_amdgcn_ds_read_tr16_b64_v4i16((lds_s16x4*)(base + 16384 + B3_OFF(kr, ch) + 8 * (lp & 1)));
        const s16x4 hi = __builtin_amdgcn_ds_read_tr16_b64_v4i16((lds_s16x4*)(base + 16384 + B3_OFF(kr + 4, ch) + 8 * (lp & 1)));
        b[ni] = __builtin_shufflevector(lo, hi, 0, 1, 2, 3, 4, 5, 6, 7);
      }
#pragma unroll
      for (int mi = 0; mi < 2; ++mi)
#pragma unroll
        for (int ni = 0; ni < 2; ++ni)
          acc[mi][ni] = __builtin_amdgcn_mfma_f32_16x16x32_bf16(b[ni], a[mi], acc[mi][ni], 0, 0, 0);
    }
    if (t < 15) swrite(t + 1, (t + 1) & 1);
    __syncthreads();
  }
  const float* mf = reinterpret_cast<const float*>(ws + WS_MODF);
  const float* wr1 = reinterpret_cast<const float*>(ws + WS_WR1);
  u16* x1 = reinterpret_cast<u16*>(ws + WS_X1);
  float* rs = reinterpret_cast<float*>(ws + WS_RS2);
#pragma unroll
  for (int mi = 0; mi < 2; ++mi) {
    const int m = row0 + wm * 32 + mi * 16 + l15;
    float s1 = 0.f, s2 = 0.f, s3 = 0.f;
#pragma unroll
    for (int ni = 0; ni < 2; ++ni) {
      const int n = col0 + wn * 32 + ni * 16 + lg * 4;
      const u32x2 xv = *reinterpret_cast<const u32x2*>(x + (size_t)m * D_ + n);
      const f32x4 g = *reinterpret_cast<const f32x4*>(mf + 2048 + n);
      const f32x4 w = *reinterpret_cast<const f32x4*>(wr1 + n);
      float v[4];
      v[0] = rbf(lo2f(xv[0]) + rbf(g[0] * rbf(acc[mi][ni][0])));
      v[1] = rbf(hi2f(xv[0]) + rbf(g[1] * rbf(acc[mi][ni][1])));
      v[2] = rbf(lo2f(xv[1]) + rbf(g[2] * rbf(acc[mi][ni][2])));
      v[3] = rbf(hi2f(xv[1]) + rbf(g[3] * rbf(acc[mi][ni][3])));
      u32x2 ov = {pk2(v[0], v[1]), pk2(v[2], v[3])};
      *reinterpret_cast<u32x2*>(x1 + (size_t)m * D_ + n) = ov;
#pragma unroll
      for (int j = 0; j < 4; ++j) { s1 += v[j]; s2 += v[j] * v[j]; s3 += v[j] * w[j]; }
    }
    s1 += __shfl_xor(s1, 16); s1 += __shfl_xor(s1, 32);
    s2 += __shfl_xor(s2, 16); s2 += __shfl_xor(s2, 32);
    s3 += __shfl_xor(s3, 16); s3 += __shfl_xor(s3, 32);
    if (lg == 0) { atomicAdd(rs + m, s1); atomicAdd(rs + 2048 + m, s2); atomicAdd(rs + 4096 + m, s3); }
  }
}

__device__ __forceinline__ void row_stats2(const float* rs, int row, float& m, float& rstd) {
  const float mean = rs[row] * (1.f / D_);
  float var = rs[2048 + row] * (1.f / D_) - mean * mean;
  var = fmaxf(var, 0.f);
  m = rbf(mean);
  rstd = 1.f / rbf(sqrtf(rbf(rbf(var) + 1e-5f)));
}

__global__ __launch_bounds__(256) void k_gateup(const u16* __restrict__ wg, const u16* __restrict__ wu,
                                                char* __restrict__ ws, u16* __restrict__ out) {
  const int tid = threadIdx.x, lane = tid & 63, wid = tid >> 6;
  const float* rs = reinterpret_cast<const float*>(ws + WS_RS2);
  const float* mf = reinterpret_cast<const float*>(ws + WS_MODF);
  const u16* x1 = reinterpret_cast<const u16*>(ws + WS_X1);
  if (blockIdx.x < 224) {
    __shared__ float sv[4][2];
    __shared__ int si[4][2];
    __shared__ float hs[2][256];
    __shared__ float red[4][4][64];
    const int cg = blockIdx.x % 56, ks = blockIdx.x / 56;
    const int c8 = tid & 7, kr = tid >> 3;
    float ra[8], rb[8], rc[8];
#pragma unroll
    for (int i = 0; i < 8; ++i) { ra[i] = rs[i * 256 + tid]; rb[i] = rs[2048 + i * 256 + tid]; rc[i] = rs[4096 + i * 256 + tid]; }
    const float W1 = reinterpret_cast<const float*>(ws + WS_W12)[0], W2 = reinterpret_cast<const float*>(ws + WS_W12)[1];
    const int kk = ks * 256 + tid;
    const float ma = mf[4096 + kk], ms = mf[3072 + kk];
    u32x4 g[8], u[8];
#pragma unroll
    for (int i = 0; i < 8; ++i) {
      const size_t off = (size_t)(ks * 256 + i * 32 + kr) * F_ + cg * 64 + c8 * 8;
      g[i] = *reinterpret_cast<const u32x4*>(wg + off);
      u[i] = *reinterpret_cast<const u32x4*>(wu + off);
    }
    __builtin_amdgcn_sched_barrier(0);
    float v1 = -3.0e38f, v2 = -3.0e38f; int i1 = 0x7fffffff, i2 = 0x7fffffff;
#pragma unroll
    for (int i = 0; i < 8; ++i) {
      const int row = i * 256 + tid;
      const float mean = ra[i] * (1.f / D_);
      const float var = fmaxf(rb[i] * (1.f / D_) - mean * mean, 0.f);
      const float m = rbf(mean), rstd = 1.f / rbf(sqrtf(rbf(rbf(var) + 1e-5f)));
      const float r = rbf(rstd * (rc[i] - m * W1) + W2);
      const bool b1 = r > v1, b2 = r > v2;
      v2 = b1 ? v1 : (b2 ? r : v2); i2 = b1 ? i1 : (b2 ? row : i2);
      v1 = b1 ? r : v1; i1 = b1 ? row : i1;
    }
#pragma unroll
    for (int o = 32; o > 0; o >>= 1) {
      const float a1 = __shfl_xor(v1, o), a2 = __shfl_xor(v2, o);
      const int b1 = __shfl_xor(i1, o), b2 = __shfl_xor(i2, o);
      if (a1 > v1 || (a1 == v1 && b1 < i1)) {
        if (v1 > a2 || (v1 == a2 && i1 < b2)) { v2 = v1; i2 = i1; } else { v2 = a2; i2 = b2; }
        v1 = a1; i1 = b1;
      } else if (a1 > v2 || (a1 == v2 && b1 < i2)) { v2 = a1; i2 = b1; }
    }
    if (lane == 0) { sv[wid][0] = v1; sv[wid][1] = v2; si[wid][0] = i1; si[wid][1] = i2; }
    __syncthreads();
    v1 = sv[0][0]; v2 = sv[0][1]; i1 = si[0][0]; i2 = si[0][1];
#pragma unroll
    for (int w = 1; w < 4; ++w) {
      const float a1 = sv[w][0], a2 = sv[w][1]; const int b1 = si[w][0], b2 = si[w][1];
      if (a1 > v1 || (a1 == v1 && b1 < i1)) {
        if (v1 > a2 || (v1 == a2 && i1 < b2)) { v2 = v1; i2 = i1; } else { v2 = a2; i2 = b2; }
        v1 = a1; i1 = b1;
      } else if (a1 > v2 || (a1 == v2 && b1 < i2)) { v2 = a1; i2 = b1; }
    }
    if (blockIdx.x == 0 && tid == 0) {
      int* sel = reinterpret_cast<int*>(ws + WS_SEL);
      sel[0] = i1; sel[1] = i2;
      reinterpret_cast<float*>(sel)[2] = v1; reinterpret_cast<float*>(sel)[3] = v2;
    }
    {
      const int k = kk;
      const float a = rbf(1.f + ma), s = ms;
      float m, rstd;
      row_stats2(rs, i1, m, rstd);
      hs[0][tid] = rbf((bf2f(x1[(size_t)i1 * D_ + k]) - m) * rstd * a + s);
      row_stats2(rs, i2, m, rstd);
      hs[1][tid] = rbf((bf2f(x1[(size_t)i2 * D_ + k]) - m) * rstd * a + s);
    }
    __syncthreads();
    float ag[2][8], au[2][8];
#pragma unroll
    for (int r = 0; r < 2; ++r)
#pragma unroll
      for (int j = 0; j < 8; ++j) { ag[r][j] = 0.f; au[r][j] = 0.f; }
#pragma unroll
    for (int i = 0; i < 8; ++i) {
      const float h0 = hs[0][i * 32 + kr], h1 = hs[1][i * 32 + kr];
#pragma unroll
      for (int j = 0; j < 4; ++j) {
        ag[0][2 * j] += h0 * lo2f(g[i][j]); ag[0][2 * j + 1] += h0 * hi2f(g[i][j]);
        ag[1][2 * j] += h1 * lo2f(g[i][j]); ag[1][2 * j + 1] += h1 * hi2f(g[i][j]);
        au[0][2 * j] += h0 * lo2f(u[i][j]); au[0][2 * j + 1] += h0 * hi2f(u[i][j]);
        au[1][2 * j] += h1 * lo2f(u[i][j]); au[1][2 * j + 1] += h1 * hi2f(u[i][j]);
      }
    }
#pragma unroll
    for (int r = 0; r < 2; ++r)
#pragma unroll
      for (int j = 0; j < 8; ++j) {
        float a = ag[r][j], b = au[r][j];
        a += __shfl_xor(a, 8); a += __shfl_xor(a, 16); a += __shfl_xor(a, 32);
        b += __shfl_xor(b, 8); b += __shfl_xor(b, 16); b += __shfl_xor(b, 32);
        if (lane < 8) { red[wid][r][lane * 8 + j] = a; red[wid][2 + r][lane * 8 + j] = b; }
      }
    __syncthreads();
    {
      const int q = tid >> 6, cc = tid & 63;
      const float v = red[0][q][cc] + red[1][q][cc] + red[2][q][cc] + red[3][q][cc];
      reinterpret_cast<float*>(ws + WS_GUP)[(size_t)(ks * 4 + q) * F_ + cg * 64 + cc] = v;
    }
  } else {
    const int rbase = (blockIdx.x - 224) * 8;
#pragma unroll 1
    for (int rr = wid; rr < 8; rr += 4) {
      const int row = rbase + rr;
      float m, rstd; row_stats2(rs, row, m, rstd);
#pragma unroll
      for (int half = 0; half < 2; ++half) {
        const int k = half * 512 + lane * 8;
        const u32x4 xv = *reinterpret_cast<const u32x4*>(x1 + (size_t)row * D_ + k);
        float a[8], s[8], gb[8];
        *reinterpret_cast<f32x4*>(a) = *reinterpret_cast<const f32x4*>(mf + 4096 + k);
        *reinterpret_cast<f32x4*>(a + 4) = *reinterpret_cast<const f32x4*>(mf + 4096 + k + 4);
        *reinterpret_cast<f32x4*>(s) = *reinterpret_cast<const f32x4*>(mf + 3072 + k);
        *reinterpret_cast<f32x4*>(s + 4) = *reinterpret_cast<const f32x4*>(mf + 3072 + k + 4);
        *reinterpret_cast<f32x4*>(gb) = *reinterpret_cast<const f32x4*>(mf + 5120 + k);
        *reinterpret_cast<f32x4*>(gb + 4) = *reinterpret_cast<const f32x4*>(mf + 5120 + k + 4);
        u32x4 ov;
#pragma unroll
        for (int j = 0; j < 4; ++j) {
          const float xa = lo2f(xv[j]), xb = hi2f(xv[j]);
          const float ha = rbf((xa - m) * rstd * rbf(1.f + a[2 * j]) + s[2 * j]);
          const float hb = rbf((xb - m) * rstd * rbf(1.f + a[2 * j + 1]) + s[2 * j + 1]);
          ov[j] = pk2(xa + rbf(gb[2 * j] * ha), xb + rbf(gb[2 * j + 1] * hb));
        }
        *reinterpret_cast<u32x4*>(out + (size_t)row * D_ + k) = ov;
      }
    }
  }
}

__global__ __launch_bounds__(256) void k_down(const u16* __restrict__ wd, char* __restrict__ ws, u16* __restrict__ out) {
  __shared__ float gs[2][224];
  __shared__ float red[4][2][64];
  __shared__ int last;
  const int tid = threadIdx.x, lane = tid & 63, wid = tid >> 6;
  const int cg = blockIdx.x & 15, ks = blockIdx.x >> 4;
  const int c8 = tid & 7, kr = tid >> 3;
  u32x4 w[7];
#pragma unroll
  for (int i = 0; i < 7; ++i)
    w[i] = *reinterpret_cast<const u32x4*>(wd + (size_t)(ks * 224 + i * 32 + kr) * D_ + cg * 64 + c8 * 8);
  const float* gup = reinterpret_cast<const float*>(ws + WS_GUP);
  if (tid < 224) {
    const int f = ks * 224 + tid;
#pragma unroll
    for (int r = 0; r < 2; ++r) {
      float g = 0.f, u = 0.f;
#pragma unroll
      for (int p = 0; p < 4; ++p) { g += gup[(size_t)(p * 4 + r) * F_ + f]; u += gup[(size_t)(p * 4 + 2 + r) * F_ + f]; }
      gs[r][tid] = rbf(rbf(silu_f(rbf(g))) * rbf(u));
    }
  }
  __syncthreads();
  float a0[8], a1[8];
#pragma unroll
  for (int j = 0; j < 8; ++j) { a0[j] = 0.f; a1[j] = 0.f; }
#pragma unroll
  for (int i = 0; i < 7; ++i) {
    const float g0 = gs[0][i * 32 + kr], g1 = gs[1][i * 32 + kr];
#pragma unroll
    for (int j = 0; j < 4; ++j) {
      a0[2 * j] += g0 * lo2f(w[i][j]); a0[2 * j + 1] += g0 * hi2f(w[i][j]);
      a1[2 * j] += g1 * lo2f(w[i][j]); a1[2 * j + 1] += g1 * hi2f(w[i][j]);
    }
  }
#pragma unroll
  for (int j = 0; j < 8; ++j) {
    float a = a0[j], b = a1[j];
    a += __shfl_xor(a, 8); a += __shfl_xor(a, 16); a += __shfl_xor(a, 32);
    b += __shfl_xor(b, 8); b += __shfl_xor(b, 16); b += __shfl_xor(b, 32);
    if (lane < 8) { red[wid][0][lane * 8 + j] = a; red[wid][1][lane * 8 + j] = b; }
  }
  __syncthreads();
  float* yp = reinterpret_cast<float*>(ws + WS_YP);
  if (tid < 128) {
    const int r = tid >> 6, cc = tid & 63;
    atomicAdd(yp + (size_t)r * D_ + cg * 64 + cc, red[0][r][cc] + red[1][r][cc] + red[2][r][cc] + red[3][r][cc]);
  }
  asm volatile("s_waitcnt vmcnt(0)" ::: "memory");
  __syncthreads();
  if (tid == 0) {
    const unsigned t = atomicAdd(reinterpret_cast<unsigned*>(ws + WS_CNT) + cg, 1u);
    last = (t == 15u);
  }
  __syncthreads();
  if (last) {
    if (tid < 128) {
      const int r = tid >> 6, cc = tid & 63, n = cg * 64 + cc;
      float y = __hip_atomic_load(yp + (size_t)r * D_ + n, __ATOMIC_RELAXED, __HIP_MEMORY_SCOPE_AGENT);
      const int* sel = reinterpret_cast<const int*>(ws + WS_SEL);
      const int row = sel[r];
      const float rv = reinterpret_cast<const float*>(sel)[2 + r];
      const float sg = rbf(1.f / (1.f + __expf(-rv)));
      y = rbf(rbf(y) * sg);
      const float* rs = reinterpret_cast<const float*>(ws + WS_RS2);
      const float* mf = reinterpret_cast<const float*>(ws + WS_MODF);
      const u16* x1 = reinterpret_cast<const u16*>(ws + WS_X1);
      float m, rstd; row_stats2(rs, row, m, rstd);
      const float xv = bf2f(x1[(size_t)row * D_ + n]);
      const float h2 = rbf((xv - m) * rstd * rbf(1.f + mf[4096 + n]) + mf[3072 + n]);
      const float B = rbf(h2 + y);
      out[(size_t)row * D_ + n] = (u16)(pk2(xv + rbf(mf[5120 + n] * B), 0.f) & 0xffff);
    }
  }
}

extern "C" void kernel_launch(void* const* d_in, const int* in_sizes, int n_in, void* d_out, int out_size,
                              void* d_ws, size_t ws_size, hipStream_t stream) {
  const u16* x = (const u16*)d_in[0];
  const u16* wqkv = (const u16*)d_in[1];
  const u16* wphi = (const u16*)d_in[2];
  const u16* wo = (const u16*)d_in[3];
  const u16* wr = (const u16*)d_in[5];
  const u16* wg = (const u16*)d_in[6];
  const u16* wu = (const u16*)d_in[7];
  const u16* wd = (const u16*)d_in[8];
  const u16* c = (const u16*)d_in[9];
  const u16* wmod = (const u16*)d_in[10];
  char* ws = (char*)d_ws;
  u16* out = (u16*)d_out;
  hipFuncSetAttribute((const void*)k_qkv, hipFuncAttributeMaxDynamicSharedMemorySize, K1_LDS);
  k_prep<<<896, 256, 0, stream>>>(x, c, wmod, ws);
  k_qkv<<<256, 512, K1_LDS, stream>>>(x, wqkv, wphi, ws);
  k_o<<<257, 256, 0, stream>>>(wphi, wr, ws);
  k_wo<<<256, 512, 0, stream>>>(x, wo, ws);
  k_gateup<<<480, 256, 0, stream>>>(wg, wu, ws, out);
  k_down<<<256, 256, 0, stream>>>(wd, ws, out);
}
```

```cpp
#include <hip/hip_runtime.h>
#include <stdint.h>

typedef unsigned short u16;
typedef short bf16x8 __attribute__((ext_vector_type(8)));
typedef short s16x4 __attribute__((ext_vector_type(4)));
typedef float f32x2 __attribute__((ext_vector_type(2)));
typedef float f32x4 __attribute__((ext_vector_type(4)));
typedef float f32x16 __attribute__((ext_vector_type(16)));
typedef unsigned u32x4 __attribute__((ext_vector_type(4)));
typedef unsigned u32x2 __attribute__((ext_vector_type(2)));
typedef __bf16 bf16x2v __attribute__((ext_vector_type(2)));
typedef __attribute__((address_space(3))) s16x4 lds_s16x4;

#define S_ 2048
#define D_ 1024
#define H_ 32
#define F_ 3584

#define WS_MODP   0u
#define WS_STATS  (WS_MODP + 4u*6144u*4u)
#define WS_RS2    (WS_STATS + 2048u*8u)
#define WS_CNT    (WS_RS2 + 3u*2048u*4u)
#define WS_MODF   (WS_CNT + 256u)
#define WS_WR1    (WS_MODF + 6144u*4u)
#define WS_W12    (WS_WR1 + 1024u*4u)
#define WS_SEL    (WS_W12 + 64u)
#define WS_GUP    (WS_SEL + 64u)
#define WS_YP     (WS_GUP + 4u*2u*2u*3584u*4u)
#define WS_Q      (WS_YP + 2u*1024u*4u)
#define WS_O      (WS_Q + 2048u*1024u*2u)
#define WS_X1     (WS_O + 2048u*1024u*2u)
#define WS_KVP    (WS_X1 + 2048u*1024u*2u)
#define WS_END    (WS_KVP + 16u*32u*2112u*4u)

__device__ __forceinline__ float bf2f(u16 u) { return __uint_as_float(((unsigned)u) << 16); }
__device__ __forceinline__ unsigned pk2(float a, float b) {
  f32x2 f = {a, b}; bf16x2v h = __builtin_convertvector(f, bf16x2v); return __builtin_bit_cast(unsigned, h);
}
__device__ __forceinline__ float rbf(float a) {
  return __uint_as_float(pk2(a, 0.f) << 16);
}
__device__ __forceinline__ float lo2f(unsigned w) { return __uint_as_float(w << 16); }
__device__ __forceinline__ float hi2f(unsigned w) { return __uint_as_float(w & 0xffff0000u); }
__device__ __forceinline__ float wave_sum(float v) {
  for (int o = 32; o > 0; o >>= 1) v += __shfl_xor(v, o);
  return v;
}
__device__ __forceinline__ float silu_f(float x) { return x / (1.f + __expf(-x)); }

__global__ __launch_bounds__(256) void k_prep(const u16* __restrict__ x, const u16* __restrict__ c,
                                              const u16* __restrict__ wmod, char* __restrict__ ws) {
  const int tid = threadIdx.x, lane = tid & 63, wid = tid >> 6;
  if (blockIdx.x < 384) {
    __shared__ float red[4][64];
    const int cg = blockIdx.x % 96, ks = blockIdx.x / 96;
    const int c8 = tid & 7, kr = tid >> 3;
    u32x4 w[8];
    float sc[8];
#pragma unroll
    for (int i = 0; i < 8; ++i) {
      const int k = ks * 256 + i * 32 + kr;
      w[i] = *reinterpret_cast<const u32x4*>(wmod + (size_t)k * 6144 + cg * 64 + c8 * 8);
      sc[i] = rbf(silu_f(bf2f(c[k])));
    }
    float acc[8] = {0, 0, 0, 0, 0, 0, 0, 0};
#pragma unroll
    for (int i = 0; i < 8; ++i)
#pragma unroll
      for (int j = 0; j < 4; ++j) { acc[2 * j] += sc[i] * lo2f(w[i][j]); acc[2 * j + 1] += sc[i] * hi2f(w[i][j]); }
#pragma unroll
    for (int j = 0; j < 8; ++j) {
      float v = acc[j];
      v += __shfl_xor(v, 8); v += __shfl_xor(v, 16); v += __shfl_xor(v, 32);
      acc[j] = v;
    }
    if (lane < 8)
#pragma unroll
      for (int j = 0; j < 8; ++j) red[wid][lane * 8 + j] = acc[j];
    __syncthreads();
    if (tid < 64) {
      float v = red[0][tid] + red[1][tid] + red[2][tid] + red[3][tid];
      reinterpret_cast<float*>(ws + WS_MODP)[ks * 6144 + cg * 64 + tid] = v;
    }
  } else {
    const int row = (blockIdx.x - 384) * 4 + wid;
    const u32x4 a = *reinterpret_cast<const u32x4*>(x + (size_t)row * D_ + lane * 8);
    const u32x4 b = *reinterpret_cast<const u32x4*>(x + (size_t)row * D_ + 512 + lane * 8);
    float s = 0.f;
#pragma unroll
    for (int j = 0; j < 4; ++j) s += lo2f(a[j]) + hi2f(a[j]) + lo2f(b[j]) + hi2f(b[j]);
    const float mean = wave_sum(s) * (1.f / D_);
    float q = 0.f;
#pragma unroll
    for (int j = 0; j < 4; ++j) {
      float d;
      d = lo2f(a[j]) - mean; q += d * d; d = hi2f(a[j]) - mean; q += d * d;
      d = lo2f(b[j]) - mean; q += d * d; d = hi2f(b[j]) - mean; q += d * d;
    }
    const float var = wave_sum(q) * (1.f / D_);
    if (lane == 0) {
      const float sd = rbf(sqrtf(rbf(rbf(var) + 1e-5f)));
      float* st = reinterpret_cast<float*>(ws + WS_STATS);
      st[row * 2] = rbf(mean);
      st[row * 2 + 1] = 1.f / sd;
      float* rs = reinterpret_cast<float*>(ws + WS_RS2);
      rs[row] = 0.f; rs[2048 + row] = 0.f; rs[4096 + row] = 0.f;
    }
    if (blockIdx.x == 384 && tid < 64) reinterpret_cast<unsigned*>(ws + WS_CNT)[tid] = 0u;
    {
      const int bi = blockIdx.x - 384;
      if (bi < 2) reinterpret_cast<f32x4*>(ws + WS_YP)[bi * 256 + tid] = f32x4{0.f, 0.f, 0.f, 0.f};
    }
  }
}

#define A_OFF(row, ch) ((row) * 128 + ((((ch) ^ ((row) & 7))) << 4))
#define B_OFF(row, ch) ((((row) >> 3) * 6 + ((ch) >> 2)) * 512 + ((row) & 7) * 64 + ((((ch) & 3) ^ (((row) >> 2) & 3)) << 4))
#define K1_STAGE 40960
#define K1_MOD (2 * K1_STAGE)
#define K1_WPHI (K1_MOD + 8192)
#define K1_LDS (K1_WPHI + 4096)
#define C_STRIDE 400
#define PHI_OFF (128 * C_STRIDE)
#define PHI_STRIDE 144

__global__ __launch_bounds__(512) void k_qkv(const u16* __restrict__ x, const u16* __restrict__ wqkv,
                                             const u16* __restrict__ wphi, char* __restrict__ ws) {
  extern __shared__ __attribute__((aligned(16))) char smem[];
  const int tid = threadIdx.x, lane = tid & 63, wid = tid >> 6;
  const int xcd = blockIdx.x & 7, idx = blockIdx.x >> 3;
  const int mt = (xcd & 1) * 8 + (idx & 7), hp = (xcd >> 1) * 4 + (idx >> 3);
  const int row0 = mt * 128;
  float* lmod = reinterpret_cast<float*>(smem + K1_MOD);
  const int sr = tid >> 3, sch = tid & 7;
  const float* st = reinterpret_cast<const float*>(ws + WS_STATS);
  const float m0 = st[(row0 + sr) * 2], r0 = st[(row0 + sr) * 2 + 1];
  const float m1 = st[(row0 + sr + 64) * 2], r1 = st[(row0 + sr + 64) * 2 + 1];
  const u16* xa0 = x + (size_t)(row0 + sr) * D_ + sch * 8;
  const u16* xa1 = xa0 + 64 * D_;
  const u16* wb = wqkv + (size_t)sr * 3072 + hp * 64 + sch * 8;

  u32x4 rg[4][5];
  auto gload = [&](int t) {
    u32x4* g = rg[t & 3];
    g[0] = *reinterpret_cast<const u32x4*>(xa0 + t * 64);
    g[1] = *reinterpret_cast<const u32x4*>(xa1 + t * 64);
    const u16* p = wb + (size_t)t * 64 * 3072;
    g[2] = *reinterpret_cast<const u32x4*>(p);
    g[3] = *reinterpret_cast<const u32x4*>(p + 1024);
    g[4] = *reinterpret_cast<const u32x4*>(p + 2048);
  };
  auto swrite = [&](int t, int buf) {
    char* base = smem + buf * K1_STAGE;
    const u32x4 ga0 = rg[t & 3][0], ga1 = rg[t & 3][1], gb0 = rg[t & 3][2], gb1 = rg[t & 3][3], gb2 = rg[t & 3][4];
    const f32x4 a0 = *reinterpret_cast<const f32x4*>(lmod + t * 64 + sch * 8);
    const f32x4 a1 = *reinterpret_cast<const f32x4*>(lmod + t * 64 + sch * 8 + 4);
    const f32x4 s0 = *reinterpret_cast<const f32x4*>(lmod + 1024 + t * 64 + sch * 8);
    const f32x4 s1 = *reinterpret_cast<const f32x4*>(lmod + 1024 + t * 64 + sch * 8 + 4);
    const float av[8] = {a0[0], a0[1], a0[2], a0[3], a1[0], a1[1], a1[2], a1[3]};
    const float sv[8] = {s0[0], s0[1], s0[2], s0[3], s1[0], s1[1], s1[2], s1[3]};
    u32x4 h0, h1;
#pragma unroll
    for (int j = 0; j < 4; ++j) {
      h0[j] = pk2((lo2f(ga0[j]) - m0) * r0 * av[2 * j] + sv[2 * j], (hi2f(ga0[j]) - m0) * r0 * av[2 * j + 1] + sv[2 * j + 1]);
      h1[j] = pk2((lo2f(ga1[j]) - m1) * r1 * av[2 * j] + sv[2 * j], (hi2f(ga1[j]) - m1) * r1 * av[2 * j + 1] + sv[2 * j + 1]);
    }
    *reinterpret_cast<u32x4*>(base + A_OFF(sr, sch)) = h0;
    *reinterpret_cast<u32x4*>(base + A_OFF(sr + 64, sch)) = h1;
    *reinterpret_cast<u32x4*>(base + 16384 + B_OFF(sr, sch)) = gb0;
    *reinterpret_cast<u32x4*>(base + 16384 + B_OFF(sr, 8 + sch)) = gb1;
    *reinterpret_cast<u32x4*>(base + 16384 + B_OFF(sr, 16 + sch)) = gb2;
  };

  const int wm = wid & 1, wn = wid >> 1;
  const int l15 = lane & 15, lg = lane >> 4, lq = (lane & 15) >> 2, lp = lane & 3;
  f32x4 acc[4][3];
#pragma unroll
  for (int i = 0; i < 4; ++i)
#pragma unroll
    for (int j = 0; j < 3; ++j) acc[i][j] = f32x4{0.f, 0.f, 0.f, 0.f};

  gload(0); gload(1); gload(2);
  {
    if (tid < 256) *reinterpret_cast<u32x4*>(smem + K1_WPHI + tid * 16) = *reinterpret_cast<const u32x4*>(wphi + (size_t)hp * 2048 + tid * 8);
    const float* mp = reinterpret_cast<const float*>(ws + WS_MODP);
#pragma unroll
    for (int i = tid; i < 1024; i += 512) {
      const float sa = mp[i] + mp[6144 + i] + mp[2 * 6144 + i] + mp[3 * 6144 + i];
      const float ca = mp[1024 + i] + mp[6144 + 1024 + i] + mp[2 * 6144 + 1024 + i] + mp[3 * 6144 + 1024 + i];
      lmod[i] = rbf(1.f + rbf(ca));
      lmod[1024 + i] = rbf(sa);
    }
  }
  __syncthreads();
  swrite(0, 0);
  __syncthreads();
#pragma unroll
  for (int t = 0; t < 16; ++t) {
    if (t < 13) gload(t + 3);
    const char* base = smem + (t & 1) * K1_STAGE;
#pragma unroll
    for (int ks = 0; ks < 2; ++ks) {
      bf16x8 a[4], b[3];
#pragma unroll
      for (int mi = 0; mi < 4; ++mi) {
        const int r = wm * 64 + mi * 16 + l15;
        a[mi] = *reinterpret_cast<const bf16x8*>(base + A_OFF(r, ks * 4 + lg));
      }
#pragma unroll
      for (int ni = 0; ni < 3; ++ni) {
        const int ch = wn * 6 + ni * 2 + (lp >> 1);
        const int kr = ks * 32 + lg * 8 + lq;
        const s16x4 lo = __builtin_amdgcn_ds_read_tr16_b64_v4i16((lds_s16x4*)(base + 16384 + B_OFF(kr, ch) + 8 * (lp & 1)));
        const s16x4 hi = __builtin_amdgcn_ds_read_tr16_b64_v4i16((lds_s16x4*)(base + 16384 + B_OFF(kr + 4, ch) + 8 * (lp & 1)));
        b[ni] = __builtin_shufflevector(lo, hi, 0, 1, 2, 3, 4, 5, 6, 7);
      }
#pragma unroll
      for (int mi = 0; mi < 4; ++mi)
#pragma unroll
        for (int ni = 0; ni < 3; ++ni)
          acc[mi][ni] = __builtin_amdgcn_mfma_f32_16x16x32_bf16(b[ni], a[mi], acc[mi][ni], 0, 0, 0);
    }
    if (t < 15) swrite(t + 1, (t + 1) & 1);
    __syncthreads();
  }
#pragma unroll
  for (int mi = 0; mi < 4; ++mi)
#pragma unroll
    for (int ni = 0; ni < 3; ++ni) {
      const int m = wm * 64 + mi * 16 + l15, n = wn * 48 + ni * 16 + lg * 4;
      u32x2 v = {pk2(acc[mi][ni][0], acc[mi][ni][1]), pk2(acc[mi][ni][2], acc[mi][ni][3])};
      *reinterpret_cast<u32x2*>(smem + m * C_STRIDE + n * 2) = v;
    }
  __syncthreads();
  {
    u16* q = reinterpret_cast<u16*>(ws + WS_Q);
#pragma unroll
    for (int i = 0; i < 2; ++i) {
      const int r = sr + i * 64;
      const u32x4 v = *reinterpret_cast<const u32x4*>(smem + r * C_STRIDE + sch * 16);
      *reinterpret_cast<u32x4*>(q + (size_t)(row0 + r) * D_ + hp * 64 + sch * 8) = v;
    }
  }
  const int l31 = lane & 31, lh = lane >> 5;
  {
    const int hh = wid & 1, rt = wid >> 1;
    const u16* wp = reinterpret_cast<const u16*>(smem + K1_WPHI) + hh * 1024;
    f32x16 xp;
#pragma unroll
    for (int i = 0; i < 16; ++i) xp[i] = 0.f;
#pragma unroll
    for (int s = 0; s < 2; ++s) {
      bf16x8 a;
#pragma unroll
      for (int j = 0; j < 8; ++j) a[j] = (short)wp[(16 * s + 8 * lh + j) * 32 + l31];
      const bf16x8 b = *reinterpret_cast<const bf16x8*>(smem + (rt * 32 + l31) * C_STRIDE + (64 + hh * 32 + 16 * s + 8 * lh) * 2);
      xp = __builtin_amdgcn_mfma_f32_32x32x16_bf16(a, b, xp, 0, 0, 0);
    }
    float mx = -3.0e38f, mn = 3.0e38f;
#pragma unroll
    for (int i = 0; i < 16; ++i) { xp[i] = rbf(xp[i]); mx = fmaxf(mx, xp[i]); mn = fminf(mn, xp[i]); }
    mx = fmaxf(mx, __shfl_xor(mx, 32)); mn = fminf(mn, __shfl_xor(mn, 32));
    float ep[16], en[16], sp = 0.f, sn = 0.f;
#pragma unroll
    for (int i = 0; i < 16; ++i) { ep[i] = __expf(xp[i] - mx); sp += ep[i]; en[i] = __expf(mn - xp[i]); sn += en[i]; }
    sp += __shfl_xor(sp, 32); sn += __shfl_xor(sn, 32);
    const float ip = 1.f / sp, in_ = 1.f / sn;
    char* ph = smem + PHI_OFF + (hh * 128 + rt * 32 + l31) * PHI_STRIDE;
#pragma unroll
    for (int g = 0; g < 4; ++g) {
      u32x2 v = {pk2(ep[4 * g] * ip, ep[4 * g + 1] * ip), pk2(ep[4 * g + 2] * ip, ep[4 * g + 3] * ip)};
      *reinterpret_cast<u32x2*>(ph + (8 * g + 4 * lh) * 2) = v;
      u32x2 w = {pk2(en[4 * g] * in_, en[4 * g + 1] * in_), pk2(en[4 * g + 2] * in_, en[4 * g + 3] * in_)};
      *reinterpret_cast<u32x2*>(ph + 64 + (8 * g + 4 * lh) * 2) = w;
    }
  }
  __syncthreads();
  if (wid < 4) {
    const int hh = wid & 1, ft = wid >> 1;
    const int blk = (lane >> 4) & 1;
    f32x16 kv, zz;
#pragma unroll
    for (int i = 0; i < 16; ++i) { kv[i] = 0.f; zz[i] = 0.f; }
    bf16x8 ones;
#pragma unroll
    for (int j = 0; j < 8; ++j) ones[j] = (short)0x3f80;
#pragma unroll
    for (int s = 0; s < 8; ++s) {
      const char* pa = smem + PHI_OFF + (hh * 128 + 16 * s + 8 * lh + lq) * PHI_STRIDE + (ft * 32 + 16 * blk + 4 * lp) * 2;
      const s16x4 alo = __builtin_amdgcn_ds_read_tr16_b64_v4i16((lds_s16x4*)pa);
      const s16x4 ahi = __builtin_amdgcn_ds_read_tr16_b64_v4i16((lds_s16x4*)(pa + 4 * PHI_STRIDE));
      const bf16x8 a = __builtin_shufflevector(alo, ahi, 0, 1, 2, 3, 4, 5, 6, 7);
      const char* pb = smem + (16 * s + 8 * lh + lq) * C_STRIDE + (128 + hh * 32 + 16 * blk + 4 * lp) * 2;
      const s16x4 blo = __builtin_amdgcn_ds_read_tr16_b64_v4i16((lds_s16x4*)pb);
      const s16x4 bhi = __builtin_amdgcn_ds_read_tr16_b64_v4i16((lds_s16x4*)(pb + 4 * C_STRIDE));
      const bf16x8 b = __builtin_shufflevector(blo, bhi, 0, 1, 2, 3, 4, 5, 6, 7);
      kv = __builtin_amdgcn_mfma_f32_32x32x16_bf16(a, b, kv, 0, 0, 0);
      zz = __builtin_amdgcn_mfma_f32_32x32x16_bf16(a, ones, zz, 0, 0, 0);
    }
    float* kp = reinterpret_cast<float*>(ws + WS_KVP) + (size_t)((hp * 2 + hh) * 16 + mt) * 2112;
#pragma unroll
    for (int i = 0; i < 16; ++i) {
      const int f = ft * 32 + (i & 3) + 8 * (i >> 2) + 4 * lh;
      kp[f * 32 + l31] = kv[i];
      if (l31 == 0) kp[2048 + f] = zz[i];
    }
  }
}

__global__ __launch_bounds__(256) void k_o(const u16* __restrict__ wphi, const u16* __restrict__ wr, char* __restrict__ ws) {
  __shared__ __attribute__((aligned(16))) u16 kvt[32 * 68 + 64];
  __shared__ float zs[64];
  __shared__ float red[8];
  const int tid = threadIdx.x, lane = tid & 63, wid = tid >> 6;
  if (blockIdx.x == 256) {
    const float* mp = reinterpret_cast<const float*>(ws + WS_MODP);
    float* mf = reinterpret_cast<float*>(ws + WS_MODF);
    float* wr1 = reinterpret_cast<float*>(ws + WS_WR1);
    float s1 = 0.f, s2 = 0.f;
#pragma unroll
    for (int i = tid; i < 1024; i += 256) {
#pragma unroll
      for (int q = 0; q < 6; ++q)
        mf[q * 1024 + i] = rbf(mp[q * 1024 + i] + mp[6144 + q * 1024 + i] + mp[2 * 6144 + q * 1024 + i] + mp[3 * 6144 + q * 1024 + i]);
      const float sb = rbf(mp[3072 + i] + mp[6144 + 3072 + i] + mp[2 * 6144 + 3072 + i] + mp[3 * 6144 + 3072 + i]);
      const float cb = rbf(mp[4096 + i] + mp[6144 + 4096 + i] + mp[2 * 6144 + 4096 + i] + mp[3 * 6144 + 4096 + i]);
      const float w = bf2f(wr[i]);
      const float a = rbf(1.f + cb) * w;
      wr1[i] = a; s1 += a; s2 += sb * w;
    }
    s1 = wave_sum(s1); s2 = wave_sum(s2);
    if (lane == 0) { red[wid] = s1; red[4 + wid] = s2; }
    __syncthreads();
    if (tid == 0) {
      float* w12 = reinterpret_cast<float*>(ws + WS_W12);
      w12[0] = red[0] + red[1] + red[2] + red[3];
      w12[1] = red[4] + red[5] + red[6] + red[7];
    }
    return;
  }
  const int h = blockIdx.x & 31, rc = blockIdx.x >> 5;
  const int l31 = lane & 31, lh = lane >> 5;
  const u16* q = reinterpret_cast<const u16*>(ws + WS_Q);
  bf16x8 qb[2][2];
#pragma unroll
  for (int rt = 0; rt < 2; ++rt)
#pragma unroll
    for (int s = 0; s < 2; ++s)
      qb[rt][s] = *reinterpret_cast<const bf16x8*>(q + (size_t)(rc * 256 + wid * 64 + rt * 32 + l31) * D_ + h * 32 + 16 * s + 8 * lh);
  const u16* wp = wphi + (size_t)h * 1024;
  bf16x8 wa[2];
#pragma unroll
  for (int s = 0; s < 2; ++s)
#pragma unroll
    for (int j = 0; j < 8; ++j) wa[s][j] = (short)wp[(16 * s + 8 * lh + j) * 32 + l31];
  {
    const f32x4* kp = reinterpret_cast<const f32x4*>(ws + WS_KVP) + (size_t)h * 16 * 528;
    f32x4 s0 = {0.f, 0.f, 0.f, 0.f}, s1 = s0, s2 = s0;
#pragma unroll
    for (int m = 0; m < 16; ++m) {
      s0 += kp[m * 528 + tid];
      s1 += kp[m * 528 + 256 + tid];
    }
    if (wid == 0) {
#pragma unroll
      for (int m = 0; m < 16; ++m) s2 += kp[m * 528 + 512 + (lane & 15)];
    }
    {
      const int f = tid >> 3, d = (tid & 7) * 4;
#pragma unroll
      for (int j = 0; j < 4; ++j) {
        kvt[(d + j) * 68 + f] = (u16)(pk2(s0[j], 0.f) & 0xffff);
        kvt[(d + j) * 68 + 32 + f] = (u16)(pk2(s1[j], 0.f) & 0xffff);
      }
    }
    if (tid < 16)
#pragma unroll
      for (int j = 0; j < 4; ++j) zs[tid * 4 + j] = rbf(s2[j]);
  }
  __syncthreads();
  bf16x8 ka[4];
#pragma unroll
  for (int s = 0; s < 4; ++s) {
    const s16x4 lo = *reinterpret_cast<const s16x4*>(kvt + l31 * 68 + (s >> 1) * 32 + 16 * (s & 1) + 4 * lh);
    const s16x4 hi = *reinterpret_cast<const s16x4*>(kvt + l31 * 68 + (s >> 1) * 32 + 16 * (s & 1) + 8 + 4 * lh);
    ka[s] = __builtin_shufflevector(lo, hi, 0, 1, 2, 3, 4, 5, 6, 7);
  }
  float zp[16], zn[16];
#pragma unroll
  for (int i = 0; i < 16; ++i) { const int f = (i & 3) + 8 * (i >> 2) + 4 * lh; zp[i] = zs[f]; zn[i] = zs[32 + f]; }
  u16* o = reinterpret_cast<u16*>(ws + WS_O);
#pragma unroll
  for (int rt = 0; rt < 2; ++rt) {
    const int row = rc * 256 + wid * 64 + rt * 32 + l31;
    f32x16 xp;
#pragma unroll
    for (int i = 0; i < 16; ++i) xp[i] = 0.f;
#pragma unroll
    for (int s = 0; s < 2; ++s) {
      xp = __builtin_amdgcn_mfma_f32_32x32x16_bf16(wa[s], qb[rt][s], xp, 0, 0, 0);
    }
    float mx = -3.0e38f, mn = 3.0e38f;
#pragma unroll
    for (int i = 0; i < 16; ++i) { xp[i] = rbf(xp[i]); mx = fmaxf(mx, xp[i]); mn = fminf(mn, xp[i]); }
    mx = fmaxf(mx, __shfl_xor(mx, 32)); mn = fminf(mn, __shfl_xor(mn, 32));
    float ep[16], en[16], sp = 0.f, sn = 0.f;
#pragma unroll
    for (int i = 0; i < 16; ++i) { ep[i] = __expf(xp[i] - mx); sp += ep[i]; en[i] = __expf(mn - xp[i]); sn += en[i]; }
    sp += __shfl_xor(sp, 32); sn += __shfl_xor(sn, 32);
    const float ip = 1.f / sp, in_ = 1.f / sn;
    float den = 0.f;
    bf16x8 pb[4];
#pragma unroll
    for (int s = 0; s < 2; ++s) {
      u32x4 wpk, wnk;
#pragma unroll
      for (int j = 0; j < 4; ++j) {
        wpk[j] = pk2(ep[8 * s + 2 * j] * ip, ep[8 * s + 2 * j + 1] * ip);
        wnk[j] = pk2(en[8 * s + 2 * j] * in_, en[8 * s + 2 * j + 1] * in_);
        den += lo2f(wpk[j]) * zp[8 * s + 2 * j] + hi2f(wpk[j]) * zp[8 * s + 2 * j + 1];
        den += lo2f(wnk[j]) * zn[8 * s + 2 * j] + hi2f(wnk[j]) * zn[8 * s + 2 * j + 1];
      }
      pb[s] = __builtin_bit_cast(bf16x8, wpk);
      pb[2 + s] = __builtin_bit_cast(bf16x8, wnk);
    }
    den += __shfl_xor(den, 32);
    den = rbf(rbf(den) + 1e-6f);
    f32x16 num;
#pragma unroll
    for (int i = 0; i < 16; ++i) num[i] = 0.f;
#pragma unroll
    for (int s = 0; s < 4; ++s) num = __builtin_amdgcn_mfma_f32_32x32x16_bf16(ka[s], pb[s], num, 0, 0, 0);
#pragma unroll
    for (int g = 0; g < 4; ++g) {
      u32x2 v = {pk2(rbf(num[4 * g]) / den, rbf(num[4 * g + 1]) / den), pk2(rbf(num[4 * g + 2]) / den, rbf(num[4 * g + 3]) / den)};
      *reinterpret_cast<u32x2*>(o + (size_t)row * D_ + h * 32 + 8 * g + 4 * lh) = v;
    }
  }
}

#define B3_OFF(row, ch) ((((row) >> 3) * 2 + ((ch) >> 2)) * 512 + ((row) & 7) * 64 + ((((ch) & 3) ^ (((row) >> 2) & 3)) << 4))
#define K3_STAGE 24576
__global__ __launch_bounds__(512) void k_wo(const u16* __restrict__ x, const u16* __restrict__ wo, char* __restrict__ ws) {
  __shared__ __attribute__((aligned(16))) char smem[2 * K3_STAGE];
  const int tid = threadIdx.x, lane = tid & 63, wid = tid >> 6;
  const int xcd = blockIdx.x & 7, idx = blockIdx.x >> 3;
  const int mt = (xcd & 3) * 4 + (idx & 3), nt = (xcd >> 2) * 8 + (idx >> 2);
  const int row0 = mt * 128, col0 = nt * 64;
  const u16* o = reinterpret_cast<const u16*>(ws + WS_O);
  const int sr = tid >> 3, sch = tid & 7;
  const u16* pa0 = o + (size_t)(row0 + sr) * D_ + sch * 8;
  const u16* pa1 = pa0 + 64 * D_;
  const u16* pb = wo + (size_t)sr * D_ + col0 + sch * 8;
  u32x4 rg[4][3];
  auto gload = [&](int t) {
    rg[t & 3][0] = *reinterpret_cast<const u32x4*>(pa0 + t * 64);
    rg[t & 3][1] = *reinterpret_cast<const u32x4*>(pa1 + t * 64);
    rg[t & 3][2] = *reinterpret_cast<const u32x4*>(pb + (size_t)t * 64 * D_);
  };
  auto swrite = [&](int t, int buf) {
    char* base = smem + buf * K3_STAGE;
    const u32x4 ga0 = rg[t & 3][0], ga1 = rg[t & 3][1], gb0 = rg[t & 3][2];
    *reinterpret_cast<u32x4*>(base + A_OFF(sr, sch)) = ga0;
    *reinterpret_cast<u32x4*>(base + A_OFF(sr + 64, sch)) = ga1;
    *reinterpret_cast<u32x4*>(base + 16384 + B3_OFF(sr, sch)) = gb0;
  };
  const int wm = wid & 3, wn = wid >> 2;
  const int l15 = lane & 15, lg = lane >> 4, lq = (lane & 15) >> 2, lp = lane & 3;
  f32x4 acc[2][2];
#pragma unroll
  for (int i = 0; i < 2; ++i)
#pragma unroll
    for (int j = 0; j < 2; ++j) acc[i][j] = f32x4{0.f, 0.f, 0.f, 0.f};
  gload(0); gload(1); gload(2);
  swrite(0, 0);
  __syncthreads();
#pragma unroll
  for (int t = 0; t < 16; ++t) {
    if (t < 13) gload(t + 3);
    const char* base = smem + (t & 1) * K3_STAGE;
#pragma unroll
    for (int ks = 0; ks < 2; ++ks) {
      bf16x8 a[2], b[2];
#pragma unroll
      for (int mi = 0; mi < 2; ++mi) a[mi] = *reinterpret_cast<const bf16x8*>(base + A_OFF(wm * 32 + mi * 16 + l15, ks * 4 + lg));
#pragma unroll
      for (int ni = 0; ni < 2; ++ni) {
        const int ch = wn * 4 + ni * 2 + (lp >> 1);
        const int kr = ks * 32 + lg * 8 + lq;
        const s16x4 lo = __builtin_amdgcn_ds_read_tr16_b64_v4i16((lds_s16x4*)(base + 16384 + B3_OFF(kr, ch) + 8 * (lp & 1)));
        const s16x4 hi = __builtin_amdgcn_ds_read_tr16_b64_v4i16((lds_s16x4*)(base + 16384 + B3_OFF(kr + 4, ch) + 8 * (lp & 1)));
        b[ni] = __builtin_shufflevector(lo, hi, 0, 1, 2, 3, 4, 5, 6, 7);
      }
#pragma unroll
      for (int mi = 0; mi < 2; ++mi)
#pragma unroll
        for (int ni = 0; ni < 2; ++ni)
          acc[mi][ni] = __builtin_amdgcn_mfma_f32_16x16x32_bf16(b[ni], a[mi], acc[mi][ni], 0, 0, 0);
    }
    if (t < 15) swrite(t + 1, (t + 1) & 1);
    __syncthreads();
  }
  const float* mf = reinterpret_cast<const float*>(ws + WS_MODF);
  const float* wr1 = reinterpret_cast<const float*>(ws + WS_WR1);
  u16* x1 = reinterpret_cast<u16*>(ws + WS_X1);
  float* rs = reinterpret_cast<float*>(ws + WS_RS2);
#pragma unroll
  for (int mi = 0; mi < 2; ++mi) {
    const int m = row0 + wm * 32 + mi * 16 + l15;
    float s1 = 0.f, s2 = 0.f, s3 = 0.f;
#pragma unroll
    for (int ni = 0; ni < 2; ++ni) {
      const int n = col0 + wn * 32 + ni * 16 + lg * 4;
      const u32x2 xv = *reinterpret_cast<const u32x2*>(x + (size_t)m * D_ + n);
      const f32x4 g = *reinterpret_cast<const f32x4*>(mf + 2048 + n);
      const f32x4 w = *reinterpret_cast<const f32x4*>(wr1 + n);
      float v[4];
      v[0] = rbf(lo2f(xv[0]) + rbf(g[0] * rbf(acc[mi][ni][0])));
      v[1] = rbf(hi2f(xv[0]) + rbf(g[1] * rbf(acc[mi][ni][1])));
      v[2] = rbf(lo2f(xv[1]) + rbf(g[2] * rbf(acc[mi][ni][2])));
      v[3] = rbf(hi2f(xv[1]) + rbf(g[3] * rbf(acc[mi][ni][3])));
      u32x2 ov = {pk2(v[0], v[1]), pk2(v[2], v[3])};
      *reinterpret_cast<u32x2*>(x1 + (size_t)m * D_ + n) = ov;
#pragma unroll
      for (int j = 0; j < 4; ++j) { s1 += v[j]; s2 += v[j] * v[j]; s3 += v[j] * w[j]; }
    }
    s1 += __shfl_xor(s1, 16); s1 += __shfl_xor(s1, 32);
    s2 += __shfl_xor(s2, 16); s2 += __shfl_xor(s2, 32);
    s3 += __shfl_xor(s3, 16); s3 += __shfl_xor(s3, 32);
    if (lg == 0) { atomicAdd(rs + m, s1); atomicAdd(rs + 2048 + m, s2); atomicAdd(rs + 4096 + m, s3); }
  }
}

__device__ __forceinline__ void row_stats2(const float* rs, int row, float& m, float& rstd) {
  const float mean = rs[row] * (1.f / D_);
  float var = rs[2048 + row] * (1.f / D_) - mean * mean;
  var = fmaxf(var, 0.f);
  m = rbf(mean);
  rstd = 1.f / rbf(sqrtf(rbf(rbf(var) + 1e-5f)));
}

__device__ __forceinline__ void st_agent(float* p, float v) { __hip_atomic_store(p, v, __ATOMIC_RELAXED, __HIP_MEMORY_SCOPE_AGENT); }
__device__ __forceinline__ float ld_agent(const float* p) { return __hip_atomic_load(p, __ATOMIC_RELAXED, __HIP_MEMORY_SCOPE_AGENT); }
__device__ __forceinline__ void signal_counter(unsigned* cnt) {
  asm volatile("s_waitcnt vmcnt(0)" ::: "memory");
  __syncthreads();
  if (threadIdx.x == 0) __hip_atomic_fetch_add(cnt, 1u, __ATOMIC_RELAXED, __HIP_MEMORY_SCOPE_AGENT);
}
__device__ __forceinline__ void wait_counter(unsigned* cnt, unsigned target) {
  if (threadIdx.x == 0) {
    for (unsigned spins = 0; spins < (1u << 22); ++spins) {
      if (__hip_atomic_load(cnt, __ATOMIC_RELAXED, __HIP_MEMORY_SCOPE_AGENT) >= target) break;
      __builtin_amdgcn_s_sleep(1);
    }
  }
  __syncthreads();
}

__global__ __launch_bounds__(256) void k_moe(const u16* __restrict__ wg, const u16* __restrict__ wu, const u16* __restrict__ wd,
                                             char* __restrict__ ws, u16* __restrict__ out) {
  const int tid = threadIdx.x, lane = tid & 63, wid = tid >> 6;
  const float* rs = reinterpret_cast<const float*>(ws + WS_RS2);
  const float* mf = reinterpret_cast<const float*>(ws + WS_MODF);
  const u16* x1 = reinterpret_cast<const u16*>(ws + WS_X1);
  if (blockIdx.x < 224) {
    __shared__ float sv[4][2];
    __shared__ int si[4][2];
    __shared__ float hs[2][1024];
    __shared__ float red[4][4][16];
    const int b = blockIdx.x;
    const int col0 = ((b & 7) * 28 + (b >> 3)) * 16;
    const int c2 = tid & 1, kr = tid >> 1;
    float ra[8], rb[8], rc[8];
#pragma unroll
    for (int i = 0; i < 8; ++i) { ra[i] = rs[i * 256 + tid]; rb[i] = rs[2048 + i * 256 + tid]; rc[i] = rs[4096 + i * 256 + tid]; }
    const float W1 = reinterpret_cast<const float*>(ws + WS_W12)[0], W2 = reinterpret_cast<const float*>(ws + WS_W12)[1];
    const f32x4 ma = *reinterpret_cast<const f32x4*>(mf + 4096 + tid * 4), ms = *reinterpret_cast<const f32x4*>(mf + 3072 + tid * 4);
    u32x4 g[8], u[8];
#pragma unroll
    for (int i = 0; i < 8; ++i) {
      const size_t off = (size_t)(i * 128 + kr) * F_ + col0 + c2 * 8;
      g[i] = *reinterpret_cast<const u32x4*>(wg + off);
      u[i] = *reinterpret_cast<const u32x4*>(wu + off);
    }
    const int dcol = ((b & 7) * 16 + (b >> 3)) * 8;
    u32x4 w[14];
    if (b < 128) {
#pragma unroll
      for (int i = 0; i < 14; ++i) w[i] = *reinterpret_cast<const u32x4*>(wd + (size_t)(i * 256 + tid) * D_ + dcol);
    }
    __builtin_amdgcn_sched_barrier(0);
    float v1 = -3.0e38f, v2 = -3.0e38f; int i1 = 0x7fffffff, i2 = 0x7fffffff;
#pragma unroll
    for (int i = 0; i < 8; ++i) {
      const int row = i * 256 + tid;
      const float mean = ra[i] * (1.f / D_);
      const float var = fmaxf(rb[i] * (1.f / D_) - mean * mean, 0.f);
      const float m = rbf(mean), rstd = 1.f / rbf(sqrtf(rbf(rbf(var) + 1e-5f)));
      const float r = rbf(rstd * (rc[i] - m * W1) + W2);
      const bool b1 = r > v1, b2 = r > v2;
      v2 = b1 ? v1 : (b2 ? r : v2); i2 = b1 ? i1 : (b2 ? row : i2);
      v1 = b1 ? r : v1; i1 = b1 ? row : i1;
    }
#pragma unroll
    for (int o = 32; o > 0; o >>= 1) {
      const float a1 = __shfl_xor(v1, o), a2 = __shfl_xor(v2, o);
      const int b1 = __shfl_xor(i1, o), b2 = __shfl_xor(i2, o);
      if (a1 > v1 || (a1 == v1 && b1 < i1)) {
        if (v1 > a2 || (v1 == a2 && i1 < b2)) { v2 = v1; i2 = i1; } else { v2 = a2; i2 = b2; }
        v1 = a1; i1 = b1;
      } else if (a1 > v2 || (a1 == v2 && b1 < i2)) { v2 = a1; i2 = b1; }
    }
    if (lane == 0) { sv[wid][0] = v1; sv[wid][1] = v2; si[wid][0] = i1; si[wid][1] = i2; }
    __syncthreads();
    v1 = sv[0][0]; v2 = sv[0][1]; i1 = si[0][0]; i2 = si[0][1];
#pragma unroll
    for (int q = 1; q < 4; ++q) {
      const float a1 = sv[q][0], a2 = sv[q][1]; const int b1 = si[q][0], b2 = si[q][1];
      if (a1 > v1 || (a1 == v1 && b1 < i1)) {
        if (v1 > a2 || (v1 == a2 && i1 < b2)) { v2 = v1; i2 = i1; } else { v2 = a2; i2 = b2; }
        v1 = a1; i1 = b1;
      } else if (a1 > v2 || (a1 == v2 && b1 < i2)) { v2 = a1; i2 = b1; }
    }
    float m1, rs1, m2, rs2;
    row_stats2(rs, i1, m1, rs1);
    row_stats2(rs, i2, m2, rs2);
    {
      const u32x2 xa = *reinterpret_cast<const u32x2*>(x1 + (size_t)i1 * D_ + tid * 4);
      const u32x2 xb = *reinterpret_cast<const u32x2*>(x1 + (size_t)i2 * D_ + tid * 4);
      const float fa[4] = {lo2f(xa[0]), hi2f(xa[0]), lo2f(xa[1]), hi2f(xa[1])};
      const float fb[4] = {lo2f(xb[0]), hi2f(xb[0]), lo2f(xb[1]), hi2f(xb[1])};
#pragma unroll
      for (int j = 0; j < 4; ++j) {
        const float a = rbf(1.f + ma[j]);
        hs[0][tid * 4 + j] = rbf((fa[j] - m1) * rs1 * a + ms[j]);
        hs[1][tid * 4 + j] = rbf((fb[j] - m2) * rs2 * a + ms[j]);
      }
    }
    float fx = 0.f, fcb = 0.f, fsb = 0.f, fgb = 0.f;
    if (b < 128 && tid < 16) {
      const int r = tid >> 3, n = dcol + (tid & 7);
      fx = bf2f(x1[(size_t)(r ? i2 : i1) * D_ + n]);
      fcb = mf[4096 + n]; fsb = mf[3072 + n]; fgb = mf[5120 + n];
    }
    __syncthreads();
    float ag[2][8], au[2][8];
#pragma unroll
    for (int r = 0; r < 2; ++r)
#pragma unroll
      for (int j = 0; j < 8; ++j) { ag[r][j] = 0.f; au[r][j] = 0.f; }
#pragma unroll
    for (int i = 0; i < 8; ++i) {
      const float h0 = hs[0][i * 128 + kr], h1 = hs[1][i * 128 + kr];
#pragma unroll
      for (int j = 0; j < 4; ++j) {
        ag[0][2 * j] += h0 * lo2f(g[i][j]); ag[0][2 * j + 1] += h0 * hi2f(g[i][j]);
        ag[1][2 * j] += h1 * lo2f(g[i][j]); ag[1][2 * j + 1] += h1 * hi2f(g[i][j]);
        au[0][2 * j] += h0 * lo2f(u[i][j]); au[0][2 * j + 1] += h0 * hi2f(u[i][j]);
        au[1][2 * j] += h1 * lo2f(u[i][j]); au[1][2 * j + 1] += h1 * hi2f(u[i][j]);
      }
    }
#pragma unroll
    for (int r = 0; r < 2; ++r)
#pragma unroll
      for (int j = 0; j < 8; ++j) {
        float a = ag[r][j], c = au[r][j];
#pragma unroll
        for (int o = 2; o < 64; o <<= 1) { a += __shfl_xor(a, o); c += __shfl_xor(c, o); }
        if (lane < 2) { red[wid][r][lane * 8 + j] = a; red[wid][2 + r][lane * 8 + j] = c; }
      }
    __syncthreads();
    float* gu = reinterpret_cast<float*>(ws + WS_GUP);
    if (tid < 32) {
      const int r = tid >> 4, cc = tid & 15;
      const float gv = red[0][r][cc] + red[1][r][cc] + red[2][r][cc] + red[3][r][cc];
      const float uv = red[0][2 + r][cc] + red[1][2 + r][cc] + red[2][2 + r][cc] + red[3][2 + r][cc];
      st_agent(gu + r * F_ + col0 + cc, rbf(rbf(silu_f(rbf(gv))) * rbf(uv)));
    }
    unsigned* cnt = reinterpret_cast<unsigned*>(ws + WS_CNT);
    signal_counter(cnt);
    if (b >= 128) return;
    wait_counter(cnt, 224u);
    float a0[8], a1[8];
#pragma unroll
    for (int j = 0; j < 8; ++j) { a0[j] = 0.f; a1[j] = 0.f; }
    float g0[14], g1[14];
#pragma unroll
    for (int i = 0; i < 14; ++i) { g0[i] = ld_agent(gu + i * 256 + tid); g1[i] = ld_agent(gu + F_ + i * 256 + tid); }
#pragma unroll
    for (int i = 0; i < 14; ++i)
#pragma unroll
      for (int j = 0; j < 4; ++j) {
        a0[2 * j] += g0[i] * lo2f(w[i][j]); a0[2 * j + 1] += g0[i] * hi2f(w[i][j]);
        a1[2 * j] += g1[i] * lo2f(w[i][j]); a1[2 * j + 1] += g1[i] * hi2f(w[i][j]);
      }
#pragma unroll
    for (int j = 0; j < 8; ++j) {
      const float a = wave_sum(a0[j]), c = wave_sum(a1[j]);
      if (lane == 0) { red[wid][0][j] = a; red[wid][1][j] = c; }
    }
    __syncthreads();
    if (tid < 16) {
      const int r = tid >> 3, j = tid & 7;
      float y = red[0][r][j] + red[1][r][j] + red[2][r][j] + red[3][r][j];
      const float rv = r ? v2 : v1;
      const float sg = rbf(1.f / (1.f + __expf(-rv)));
      y = rbf(rbf(y) * sg);
      const float h2 = rbf((fx - (r ? m2 : m1)) * (r ? rs2 : rs1) * rbf(1.f + fcb) + fsb);
      const float B = rbf(h2 + y);
      out[(size_t)(r ? i2 : i1) * D_ + dcol + j] = (u16)(pk2(fx + rbf(fgb * B), 0.f) & 0xffff);
    }
  } else {
    __shared__ float sv[4][2];
    __shared__ int si[4][2];
    float v1 = -3.0e38f, v2 = -3.0e38f; int i1 = 0x7fffffff, i2 = 0x7fffffff;
    {
      float ra[8], rb[8], rc[8];
#pragma unroll
      for (int i = 0; i < 8; ++i) { ra[i] = rs[i * 256 + tid]; rb[i] = rs[2048 + i * 256 + tid]; rc[i] = rs[4096 + i * 256 + tid]; }
      const float W1 = reinterpret_cast<const float*>(ws + WS_W12)[0], W2 = reinterpret_cast<const float*>(ws + WS_W12)[1];
#pragma unroll
      for (int i = 0; i < 8; ++i) {
        const int row = i * 256 + tid;
        const float mean = ra[i] * (1.f / D_);
        const float var = fmaxf(rb[i] * (1.f / D_) - mean * mean, 0.f);
        const float m = rbf(mean), rstd = 1.f / rbf(sqrtf(rbf(rbf(var) + 1e-5f)));
        const float r = rbf(rstd * (rc[i] - m * W1) + W2);
        const bool b1 = r > v1, b2 = r > v2;
        v2 = b1 ? v1 : (b2 ? r : v2); i2 = b1 ? i1 : (b2 ? row : i2);
        v1 = b1 ? r : v1; i1 = b1 ? row : i1;
      }
#pragma unroll
      for (int o = 32; o > 0; o >>= 1) {
        const float a1 = __shfl_xor(v1, o), a2 = __shfl_xor(v2, o);
        const int b1 = __shfl_xor(i1, o), b2 = __shfl_xor(i2, o);
        if (a1 > v1 || (a1 == v1 && b1 < i1)) {
          if (v1 > a2 || (v1 == a2 && i1 < b2)) { v2 = v1; i2 = i1; } else { v2 = a2; i2 = b2; }
          v1 = a1; i1 = b1;
        } else if (a1 > v2 || (a1 == v2 && b1 < i2)) { v2 = a1; i2 = b1; }
      }
      if (lane == 0) { sv[wid][0] = v1; sv[wid][1] = v2; si[wid][0] = i1; si[wid][1] = i2; }
      __syncthreads();
      v1 = sv[0][0]; v2 = sv[0][1]; i1 = si[0][0]; i2 = si[0][1];
#pragma unroll
      for (int q = 1; q < 4; ++q) {
        const float a1 = sv[q][0], a2 = sv[q][1]; const int b1 = si[q][0], b2 = si[q][1];
        if (a1 > v1 || (a1 == v1 && b1 < i1)) {
          if (v1 > a2 || (v1 == a2 && i1 < b2)) { v2 = v1; i2 = i1; } else { v2 = a2; i2 = b2; }
          v1 = a1; i1 = b1;
        } else if (a1 > v2 || (a1 == v2 && b1 < i2)) { v2 = a1; i2 = b1; }
      }
    }
    const int rbase = (blockIdx.x - 224) * 8;
#pragma unroll 1
    for (int rr = wid; rr < 8; rr += 4) {
      const int row = rbase + rr;
      if (row == i1 || row == i2) continue;
      float m, rstd; row_stats2(rs, row, m, rstd);
#pragma unroll
      for (int half = 0; half < 2; ++half) {
        const int k = half * 512 + lane * 8;
        const u32x4 xv = *reinterpret_cast<const u32x4*>(x1 + (size_t)row * D_ + k);
        float a[8], sb[8], gb[8];
        *reinterpret_cast<f32x4*>(a) = *reinterpret_cast<const f32x4*>(mf + 4096 + k);
        *reinterpret_cast<f32x4*>(a + 4) = *reinterpret_cast<const f32x4*>(mf + 4096 + k + 4);
        *reinterpret_cast<f32x4*>(sb) = *reinterpret_cast<const f32x4*>(mf + 3072 + k);
        *reinterpret_cast<f32x4*>(sb + 4) = *reinterpret_cast<const f32x4*>(mf + 3072 + k + 4);
        *reinterpret_cast<f32x4*>(gb) = *reinterpret_cast<const f32x4*>(mf + 5120 + k);
        *reinterpret_cast<f32x4*>(gb + 4) = *reinterpret_cast<const f32x4*>(mf + 5120 + k + 4);
        u32x4 ov;
#pragma unroll
        for (int j = 0; j < 4; ++j) {
          const float xa = lo2f(xv[j]), xb = hi2f(xv[j]);
          const float ha = rbf((xa - m) * rstd * rbf(1.f + a[2 * j]) + sb[2 * j]);
          const float hb = rbf((xb - m) * rstd * rbf(1.f + a[2 * j + 1]) + sb[2 * j + 1]);
          ov[j] = pk2(xa + rbf(gb[2 * j] * ha), xb + rbf(gb[2 * j + 1] * hb));
        }
        *reinterpret_cast<u32x4*>(out + (size_t)row * D_ + k) = ov;
      }
    }
  }
}

extern "C" void kernel_launch(void* const* d_in, const int* in_sizes, int n_in, void* d_out, int out_size,
                              void* d_ws, size_t ws_size, hipStream_t stream) {
  const u16* x = (const u16*)d_in[0];
  const u16* wqkv = (const u16*)d_in[1];
  const u16* wphi = (const u16*)d_in[2];
  const u16* wo = (const u16*)d_in[3];
  const u16* wr = (const u16*)d_in[5];
  const u16* wg = (const u16*)d_in[6];
  const u16* wu = (const u16*)d_in[7];
  const u16* wd = (const u16*)d_in[8];
  const u16* c = (const u16*)d_in[9];
  const u16* wmod = (const u16*)d_in[10];
  char* ws = (char*)d_ws;
  u16* out = (u16*)d_out;
  hipFuncSetAttribute((const void*)k_qkv, hipFuncAttributeMaxDynamicSharedMemorySize, K1_LDS);
  k_prep<<<896, 256, 0, stream>>>(x, c, wmod, ws);
  k_qkv<<<256, 512, K1_LDS, stream>>>(x, wqkv, wphi, ws);
  k_o<<<257, 256, 0, stream>>>(wphi, wr, ws);
  k_wo<<<256, 512, 0, stream>>>(x, wo, ws);
  k_moe<<<480, 256, 0, stream>>>(wg, wu, wd, ws, out);
}
```

```cpp
#include <hip/hip_runtime.h>
#include <stdint.h>

typedef unsigned short u16;
typedef short bf16x8 __attribute__((ext_vector_type(8)));
typedef short s16x4 __attribute__((ext_vector_type(4)));
typedef float f32x2 __attribute__((ext_vector_type(2)));
typedef float f32x4 __attribute__((ext_vector_type(4)));
typedef float f32x16 __attribute__((ext_vector_type(16)));
typedef unsigned u32x4 __attribute__((ext_vector_type(4)));
typedef unsigned u32x2 __attribute__((ext_vector_type(2)));
typedef __bf16 bf16x2v __attribute__((ext_vector_type(2)));
typedef __attribute__((address_space(3))) s16x4 lds_s16x4;

#define S_ 2048
#define D_ 1024
#define H_ 32
#define F_ 3584

#define WS_MODP   0u
#define WS_STATS  (WS_MODP + 4u*6144u*4u)
#define WS_RS2    (WS_STATS + 2048u*8u)
#define WS_CNT    (WS_RS2 + 3u*2048u*4u)
#define WS_MODF   (WS_CNT + 256u)
#define WS_WR1    (WS_MODF + 6144u*4u)
#define WS_W12    (WS_WR1 + 1024u*4u)
#define WS_SEL    (WS_W12 + 64u)
#define WS_GUP    (WS_SEL + 64u)
#define WS_YP     (WS_GUP + 4u*2u*2u*3584u*4u)
#define WS_Q      (WS_YP + 2u*1024u*4u)
#define WS_O      (WS_Q + 2048u*1024u*2u)
#define WS_X1     (WS_O + 2048u*1024u*2u)
#define WS_KVP    (WS_X1 + 2048u*1024u*2u)
#define WS_END    (WS_KVP + 16u*32u*2112u*4u)

__device__ __forceinline__ float bf2f(u16 u) { return __uint_as_float(((unsigned)u) << 16); }
__device__ __forceinline__ unsigned pk2(float a, float b) {
  f32x2 f = {a, b}; bf16x2v h = __builtin_convertvector(f, bf16x2v); return __builtin_bit_cast(unsigned, h);
}
__device__ __forceinline__ float rbf(float a) {
  return __uint_as_float(pk2(a, 0.f) << 16);
}
__device__ __forceinline__ float lo2f(unsigned w) { return __uint_as_float(w << 16); }
__device__ __forceinline__ float hi2f(unsigned w) { return __uint_as_float(w & 0xffff0000u); }
__device__ __forceinline__ float wave_sum(float v) {
  for (int o = 32; o > 0; o >>= 1) v += __shfl_xor(v, o);
  return v;
}
__device__ __forceinline__ float silu_f(float x) { return x / (1.f + __expf(-x)); }

__global__ __launch_bounds__(256) void k_prep(const u16* __restrict__ x, const u16* __restrict__ c,
                                              const u16* __restrict__ wmod, char* __restrict__ ws) {
  const int tid = threadIdx.x, lane = tid & 63, wid = tid >> 6;
  if (blockIdx.x < 384) {
    __shared__ float red[4][64];
    const int cg = blockIdx.x % 96, ks = blockIdx.x / 96;
    const int c8 = tid & 7, kr = tid >> 3;
    u32x4 w[8];
    float sc[8];
#pragma unroll
    for (int i = 0; i < 8; ++i) {
      const int k = ks * 256 + i * 32 + kr;
      w[i] = *reinterpret_cast<const u32x4*>(wmod + (size_t)k * 6144 + cg * 64 + c8 * 8);
      sc[i] = rbf(silu_f(bf2f(c[k])));
    }
    float acc[8] = {0, 0, 0, 0, 0, 0, 0, 0};
#pragma unroll
    for (int i = 0; i < 8; ++i)
#pragma unroll
      for (int j = 0; j < 4; ++j) { acc[2 * j] += sc[i] * lo2f(w[i][j]); acc[2 * j + 1] += sc[i] * hi2f(w[i][j]); }
#pragma unroll
    for (int j = 0; j < 8; ++j) {
      float v = acc[j];
      v += __shfl_xor(v, 8); v += __shfl_xor(v, 16); v += __shfl_xor(v, 32);
      acc[j] = v;
    }
    if (lane < 8)
#pragma unroll
      for (int j = 0; j < 8; ++j) red[wid][lane * 8 + j] = acc[j];
    __syncthreads();
    if (tid < 64) {
      float v = red[0][tid] + red[1][tid] + red[2][tid] + red[3][tid];
      reinterpret_cast<float*>(ws + WS_MODP)[ks * 6144 + cg * 64 + tid] = v;
    }
  } else {
    const int row = (blockIdx.x - 384) * 4 + wid;
    const u32x4 a = *reinterpret_cast<const u32x4*>(x + (size_t)row * D_ + lane * 8);
    const u32x4 b = *reinterpret_cast<const u32x4*>(x + (size_t)row * D_ + 512 + lane * 8);
    float s = 0.f;
#pragma unroll
    for (int j = 0; j < 4; ++j) s += lo2f(a[j]) + hi2f(a[j]) + lo2f(b[j]) + hi2f(b[j]);
    const float mean = wave_sum(s) * (1.f / D_);
    float q = 0.f;
#pragma unroll
    for (int j = 0; j < 4; ++j) {
      float d;
      d = lo2f(a[j]) - mean; q += d * d; d = hi2f(a[j]) - mean; q += d * d;
      d = lo2f(b[j]) - mean; q += d * d; d = hi2f(b[j]) - mean; q += d * d;
    }
    const float var = wave_sum(q) * (1.f / D_);
    if (lane == 0) {
      const float sd = rbf(sqrtf(rbf(rbf(var) + 1e-5f)));
      float* st = reinterpret_cast<float*>(ws + WS_STATS);
      st[row * 2] = rbf(mean);
      st[row * 2 + 1] = 1.f / sd;
      float* rs = reinterpret_cast<float*>(ws + WS_RS2);
      rs[row] = 0.f; rs[2048 + row] = 0.f; rs[4096 + row] = 0.f;
    }
    if (blockIdx.x == 384 && tid < 64) reinterpret_cast<unsigned*>(ws + WS_CNT)[tid] = 0u;
    {
      const int bi = blockIdx.x - 384;
      if (bi < 2) reinterpret_cast<f32x4*>(ws + WS_YP)[bi * 256 + tid] = f32x4{0.f, 0.f, 0.f, 0.f};
    }
  }
}

#define A_OFF(row, ch) ((row) * 128 + ((((ch) ^ ((row) & 7))) << 4))
#define B_OFF(row, ch) ((((row) >> 3) * 6 + ((ch) >> 2)) * 512 + ((row) & 7) * 64 + ((((ch) & 3) ^ (((row) >> 2) & 3)) << 4))
#define K1_STAGE 40960
#define K1_MOD (2 * K1_STAGE)
#define K1_WPHI (K1_MOD + 8192)
#define K1_LDS (K1_WPHI + 4096)
#define C_STRIDE 400
#define PHI_OFF (128 * C_STRIDE)
#define PHI_STRIDE 144

__global__ __launch_bounds__(512) void k_qkv(const u16* __restrict__ x, const u16* __restrict__ wqkv,
                                             const u16* __restrict__ wphi, char* __restrict__ ws) {
  extern __shared__ __attribute__((aligned(16))) char smem[];
  const int tid = threadIdx.x, lane = tid & 63, wid = tid >> 6;
  const int xcd = blockIdx.x & 7, idx = blockIdx.x >> 3;
  const int mt = (xcd & 1) * 8 + (idx & 7), hp = (xcd >> 1) * 4 + (idx >> 3);
  const int row0 = mt * 128;
  float* lmod = reinterpret_cast<float*>(smem + K1_MOD);
  const int sr = tid >> 3, sch = tid & 7;
  const float* st = reinterpret_cast<const float*>(ws + WS_STATS);
  const float m0 = st[(row0 + sr) * 2], r0 = st[(row0 + sr) * 2 + 1];
  const float m1 = st[(row0 + sr + 64) * 2], r1 = st[(row0 + sr + 64) * 2 + 1];
  const u16* xa0 = x + (size_t)(row0 + sr) * D_ + sch * 8;
  const u16* xa1 = xa0 + 64 * D_;
  const u16* wb = wqkv + (size_t)sr * 3072 + hp * 64 + sch * 8;

  u32x4 rg[4][5];
  auto gload = [&](int t) {
    u32x4* g = rg[t & 3];
    g[0] = *reinterpret_cast<const u32x4*>(xa0 + t * 64);
    g[1] = *reinterpret_cast<const u32x4*>(xa1 + t * 64);
    const u16* p = wb + (size_t)t * 64 * 3072;
    g[2] = *reinterpret_cast<const u32x4*>(p);
    g[3] = *reinterpret_cast<const u32x4*>(p + 1024);
    g[4] = *reinterpret_cast<const u32x4*>(p + 2048);
  };
  auto swrite = [&](int t, int buf) {
    char* base = smem + buf * K1_STAGE;
    const u32x4 ga0 = rg[t & 3][0], ga1 = rg[t & 3][1], gb0 = rg[t & 3][2], gb1 = rg[t & 3][3], gb2 = rg[t & 3][4];
    const f32x4 a0 = *reinterpret_cast<const f32x4*>(lmod + t * 64 + sch * 8);
    const f32x4 a1 = *reinterpret_cast<const f32x4*>(lmod + t * 64 + sch * 8 + 4);
    const f32x4 s0 = *reinterpret_cast<const f32x4*>(lmod + 1024 + t * 64 + sch * 8);
    const f32x4 s1 = *reinterpret_cast<const f32x4*>(lmod + 1024 + t * 64 + sch * 8 + 4);
    const float av[8] = {a0[0], a0[1], a0[2], a0[3], a1[0], a1[1], a1[2], a1[3]};
    const float sv[8] = {s0[0], s0[1], s0[2], s0[3], s1[0], s1[1], s1[2], s1[3]};
    u32x4 h0, h1;
#pragma unroll
    for (int j = 0; j < 4; ++j) {
      h0[j] = pk2((lo2f(ga0[j]) - m0) * r0 * av[2 * j] + sv[2 * j], (hi2f(ga0[j]) - m0) * r0 * av[2 * j + 1] + sv[2 * j + 1]);
      h1[j] = pk2((lo2f(ga1[j]) - m1) * r1 * av[2 * j] + sv[2 * j], (hi2f(ga1[j]) - m1) * r1 * av[2 * j + 1] + sv[2 * j + 1]);
    }
    *reinterpret_cast<u32x4*>(base + A_OFF(sr, sch)) = h0;
    *reinterpret_cast<u32x4*>(base + A_OFF(sr + 64, sch)) = h1;
    *reinterpret_cast<u32x4*>(base + 16384 + B_OFF(sr, sch)) = gb0;
    *reinterpret_cast<u32x4*>(base + 16384 + B_OFF(sr, 8 + sch)) = gb1;
    *reinterpret_cast<u32x4*>(base + 16384 + B_OFF(sr, 16 + sch)) = gb2;
  };

  const int wm = wid & 1, wn = wid >> 1;
  const int l15 = lane & 15, lg = lane >> 4, lq = (lane & 15) >> 2, lp = lane & 3;
  f32x4 acc[4][3];
#pragma unroll
  for (int i = 0; i < 4; ++i)
#pragma unroll
    for (int j = 0; j < 3; ++j) acc[i][j] = f32x4{0.f, 0.f, 0.f, 0.f};

  gload(0); gload(1); gload(2);
  {
    if (tid < 256) *reinterpret_cast<u32x4*>(smem + K1_WPHI + tid * 16) = *reinterpret_cast<const u32x4*>(wphi + (size_t)hp * 2048 + tid * 8);
    const float* mp = reinterpret_cast<const float*>(ws + WS_MODP);
#pragma unroll
    for (int i = tid; i < 1024; i += 512) {
      const float sa = mp[i] + mp[6144 + i] + mp[2 * 6144 + i] + mp[3 * 6144 + i];
      const float ca = mp[1024 + i] + mp[6144 + 1024 + i] + mp[2 * 6144 + 1024 + i] + mp[3 * 6144 + 1024 + i];
      lmod[i] = rbf(1.f + rbf(ca));
      lmod[1024 + i] = rbf(sa);
    }
  }
  __syncthreads();
  swrite(0, 0);
  __syncthreads();
#pragma unroll
  for (int t = 0; t < 16; ++t) {
    if (t < 13) gload(t + 3);
    const char* base = smem + (t & 1) * K1_STAGE;
#pragma unroll
    for (int ks = 0; ks < 2; ++ks) {
      bf16x8 a[4], b[3];
#pragma unroll
      for (int mi = 0; mi < 4; ++mi) {
        const int r = wm * 64 + mi * 16 + l15;
        a[mi] = *reinterpret_cast<const bf16x8*>(base + A_OFF(r, ks * 4 + lg));
      }
#pragma unroll
      for (int ni = 0; ni < 3; ++ni) {
        const int ch = wn * 6 + ni * 2 + (lp >> 1);
        const int kr = ks * 32 + lg * 8 + lq;
        const s16x4 lo = __builtin_amdgcn_ds_read_tr16_b64_v4i16((lds_s16x4*)(base + 16384 + B_OFF(kr, ch) + 8 * (lp & 1)));
        const s16x4 hi = __builtin_amdgcn_ds_read_tr16_b64_v4i16((lds_s16x4*)(base + 16384 + B_OFF(kr + 4, ch) + 8 * (lp & 1)));
        b[ni] = __builtin_shufflevector(lo, hi, 0, 1, 2, 3, 4, 5, 6, 7);
      }
#pragma unroll
      for (int mi = 0; mi < 4; ++mi)
#pragma unroll
        for (int ni = 0; ni < 3; ++ni)
          acc[mi][ni] = __builtin_amdgcn_mfma_f32_16x16x32_bf16(b[ni], a[mi], acc[mi][ni], 0, 0, 0);
    }
    if (t < 15) swrite(t + 1, (t + 1) & 1);
    __syncthreads();
  }
#pragma unroll
  for (int mi = 0; mi < 4; ++mi)
#pragma unroll
    for (int ni = 0; ni < 3; ++ni) {
      const int m = wm * 64 + mi * 16 + l15, n = wn * 48 + ni * 16 + lg * 4;
      u32x2 v = {pk2(acc[mi][ni][0], acc[mi][ni][1]), pk2(acc[mi][ni][2], acc[mi][ni][3])};
      *reinterpret_cast<u32x2*>(smem + m * C_STRIDE + n * 2) = v;
    }
  __syncthreads();
  {
    u16* q = reinterpret_cast<u16*>(ws + WS_Q);
#pragma unroll
    for (int i = 0; i < 2; ++i) {
      const int r = sr + i * 64;
      const u32x4 v = *reinterpret_cast<const u32x4*>(smem + r * C_STRIDE + sch * 16);
      *reinterpret_cast<u32x4*>(q + (size_t)(row0 + r) * D_ + hp * 64 + sch * 8) = v;
    }
  }
  const int l31 = lane & 31, lh = lane >> 5;
  {
    const int hh = wid & 1, rt = wid >> 1;
    const u16* wp = reinterpret_cast<const u16*>(smem + K1_WPHI) + hh * 1024;
    f32x16 xp;
#pragma unroll
    for (int i = 0; i < 16; ++i) xp[i] = 0.f;
#pragma unroll
    for (int s = 0; s < 2; ++s) {
      bf16x8 a;
#pragma unroll
      for (int j = 0; j < 8; ++j) a[j] = (short)wp[(16 * s + 8 * lh + j) * 32 + l31];
      const bf16x8 b = *reinterpret_cast<const bf16x8*>(smem + (rt * 32 + l31) * C_STRIDE + (64 + hh * 32 + 16 * s + 8 * lh) * 2);
      xp = __builtin_amdgcn_mfma_f32_32x32x16_bf16(a, b, xp, 0, 0, 0);
    }
    float mx = -3.0e38f, mn = 3.0e38f;
#pragma unroll
    for (int i = 0; i < 16; ++i) { xp[i] = rbf(xp[i]); mx = fmaxf(mx, xp[i]); mn = fminf(mn, xp[i]); }
    mx = fmaxf(mx, __shfl_xor(mx, 32)); mn = fminf(mn, __shfl_xor(mn, 32));
    float ep[16], en[16], sp = 0.f, sn = 0.f;
#pragma unroll
    for (int i = 0; i < 16; ++i) { ep[i] = __expf(xp[i] - mx); sp += ep[i]; en[i] = __expf(mn - xp[i]); sn += en[i]; }
    sp += __shfl_xor(sp, 32); sn += __shfl_xor(sn, 32);
    const float ip = 1.f / sp, in_ = 1.f / sn;
    char* ph = smem + PHI_OFF + (hh * 128 + rt * 32 + l31) * PHI_STRIDE;
#pragma unroll
    for (int g = 0; g < 4; ++g) {
      u32x2 v = {pk2(ep[4 * g] * ip, ep[4 * g + 1] * ip), pk2(ep[4 * g + 2] * ip, ep[4 * g + 3] * ip)};
      *reinterpret_cast<u32x2*>(ph + (8 * g + 4 * lh) * 2) = v;
      u32x2 w = {pk2(en[4 * g] * in_, en[4 * g + 1] * in_), pk2(en[4 * g + 2] * in_, en[4 * g + 3] * in_)};
      *reinterpret_cast<u32x2*>(ph + 64 + (8 * g + 4 * lh) * 2) = w;
    }
  }
  __syncthreads();
  if (wid < 4) {
    const int hh = wid & 1, ft = wid >> 1;
    const int blk = (lane >> 4) & 1;
    f32x16 kv, zz;
#pragma unroll
    for (int i = 0; i < 16; ++i) { kv[i] = 0.f; zz[i] = 0.f; }
    bf16x8 ones;
#pragma unroll
    for (int j = 0; j < 8; ++j) ones[j] = (short)0x3f80;
#pragma unroll
    for (int s = 0; s < 8; ++s) {
      const char* pa = smem + PHI_OFF + (hh * 128 + 16 * s + 8 * lh + lq) * PHI_STRIDE + (ft * 32 + 16 * blk + 4 * lp) * 2;
      const s16x4 alo = __builtin_amdgcn_ds_read_tr16_b64_v4i16((lds_s16x4*)pa);
      const s16x4 ahi = __builtin_amdgcn_ds_read_tr16_b64_v4i16((lds_s16x4*)(pa + 4 * PHI_STRIDE));
      const bf16x8 a = __builtin_shufflevector(alo, ahi, 0, 1, 2, 3, 4, 5, 6, 7);
      const char* pb = smem + (16 * s + 8 * lh + lq) * C_STRIDE + (128 + hh * 32 + 16 * blk + 4 * lp) * 2;
      const s16x4 blo = __builtin_amdgcn_ds_read_tr16_b64_v4i16((lds_s16x4*)pb);
      const s16x4 bhi = __builtin_amdgcn_ds_read_tr16_b64_v4i16((lds_s16x4*)(pb + 4 * C_STRIDE));
      const bf16x8 b = __builtin_shufflevector(blo, bhi, 0, 1, 2, 3, 4, 5, 6, 7);
      kv = __builtin_amdgcn_mfma_f32_32x32x16_bf16(a, b, kv, 0, 0, 0);
      zz = __builtin_amdgcn_mfma_f32_32x32x16_bf16(a, ones, zz, 0, 0, 0);
    }
    float* kp = reinterpret_cast<float*>(ws + WS_KVP) + (size_t)((hp * 2 + hh) * 16 + mt) * 2112;
#pragma unroll
    for (int i = 0; i < 16; ++i) {
      const int f = ft * 32 + (i & 3) + 8 * (i >> 2) + 4 * lh;
      kp[f * 32 + l31] = kv[i];
      if (l31 == 0) kp[2048 + f] = zz[i];
    }
  }
}

__global__ __launch_bounds__(256) void k_o(const u16* __restrict__ wphi, const u16* __restrict__ wr, char* __restrict__ ws) {
  __shared__ __attribute__((aligned(16))) u16 kvt[32 * 68 + 64];
  __shared__ float zs[64];
  __shared__ float red[8];
  const int tid = threadIdx.x, lane = tid & 63, wid = tid >> 6;
  if (blockIdx.x == 256) {
    const float* mp = reinterpret_cast<const float*>(ws + WS_MODP);
    float* mf = reinterpret_cast<float*>(ws + WS_MODF);
    float* wr1 = reinterpret_cast<float*>(ws + WS_WR1);
    float s1 = 0.f, s2 = 0.f;
#pragma unroll
    for (int i = tid; i < 1024; i += 256) {
#pragma unroll
      for (int q = 0; q < 6; ++q)
        mf[q * 1024 + i] = rbf(mp[q * 1024 + i] + mp[6144 + q * 1024 + i] + mp[2 * 6144 + q * 1024 + i] + mp[3 * 6144 + q * 1024 + i]);
      const float sb = rbf(mp[3072 + i] + mp[6144 + 3072 + i] + mp[2 * 6144 + 3072 + i] + mp[3 * 6144 + 3072 + i]);
      const float cb = rbf(mp[4096 + i] + mp[6144 + 4096 + i] + mp[2 * 6144 + 4096 + i] + mp[3 * 6144 + 4096 + i]);
      const float w = bf2f(wr[i]);
      const float a = rbf(1.f + cb) * w;
      wr1[i] = a; s1 += a; s2 += sb * w;
    }
    s1 = wave_sum(s1); s2 = wave_sum(s2);
    if (lane == 0) { red[wid] = s1; red[4 + wid] = s2; }
    __syncthreads();
    if (tid == 0) {
      float* w12 = reinterpret_cast<float*>(ws + WS_W12);
      w12[0] = red[0] + red[1] + red[2] + red[3];
      w12[1] = red[4] + red[5] + red[6] + red[7];
    }
    return;
  }
  const int h = blockIdx.x & 31, rc = blockIdx.x >> 5;
  const int l31 = lane & 31, lh = lane >> 5;
  const u16* q = reinterpret_cast<const u16*>(ws + WS_Q);
  bf16x8 qb[2][2];
#pragma unroll
  for (int rt = 0; rt < 2; ++rt)
#pragma unroll
    for (int s = 0; s < 2; ++s)
      qb[rt][s] = *reinterpret_cast<const bf16x8*>(q + (size_t)(rc * 256 + wid * 64 + rt * 32 + l31) * D_ + h * 32 + 16 * s + 8 * lh);
  const u16* wp = wphi + (size_t)h * 1024;
  bf16x8 wa[2];
#pragma unroll
  for (int s = 0; s < 2; ++s)
#pragma unroll
    for (int j = 0; j < 8; ++j) wa[s][j] = (short)wp[(16 * s + 8 * lh + j) * 32 + l31];
  {
    const f32x4* kp = reinterpret_cast<const f32x4*>(ws + WS_KVP) + (size_t)h * 16 * 528;
    f32x4 s0 = {0.f, 0.f, 0.f, 0.f}, s1 = s0, s2 = s0;
#pragma unroll
    for (int m = 0; m < 16; ++m) {
      s0 += kp[m * 528 + tid];
      s1 += kp[m * 528 + 256 + tid];
    }
    if (wid == 0) {
#pragma unroll
      for (int m = 0; m < 16; ++m) s2 += kp[m * 528 + 512 + (lane & 15)];
    }
    {
      const int f = tid >> 3, d = (tid & 7) * 4;
#pragma unroll
      for (int j = 0; j < 4; ++j) {
        kvt[(d + j) * 68 + f] = (u16)(pk2(s0[j], 0.f) & 0xffff);
        kvt[(d + j) * 68 + 32 + f] = (u16)(pk2(s1[j], 0.f) & 0xffff);
      }
    }
    if (tid < 16)
#pragma unroll
      for (int j = 0; j < 4; ++j) zs[tid * 4 + j] = rbf(s2[j]);
  }
  __syncthreads();
  bf16x8 ka[4];
#pragma unroll
  for (int s = 0; s < 4; ++s) {
    const s16x4 lo = *reinterpret_cast<const s16x4*>(kvt + l31 * 68 + (s >> 1) * 32 + 16 * (s & 1) + 4 * lh);
    const s16x4 hi = *reinterpret_cast<const s16x4*>(kvt + l31 * 68 + (s >> 1) * 32 + 16 * (s & 1) + 8 + 4 * lh);
    ka[s] = __builtin_shufflevector(lo, hi, 0, 1, 2, 3, 4, 5, 6, 7);
  }
  float zp[16], zn[16];
#pragma unroll
  for (int i = 0; i < 16; ++i) { const int f = (i & 3) + 8 * (i >> 2) + 4 * lh; zp[i] = zs[f]; zn[i] = zs[32 + f]; }
  u16* o = reinterpret_cast<u16*>(ws + WS_O);
#pragma unroll
  for (int rt = 0; rt < 2; ++rt) {
    const int row = rc * 256 + wid * 64 + rt * 32 + l31;
    f32x16 xp;
#pragma unroll
    for (int i = 0; i < 16; ++i) xp[i] = 0.f;
#pragma unroll
    for (int s = 0; s < 2; ++s) {
      xp = __builtin_amdgcn_mfma_f32_32x32x16_bf16(wa[s], qb[rt][s], xp, 0, 0, 0);
    }
    float mx = -3.0e38f, mn = 3.0e38f;
#pragma unroll
    for (int i = 0; i < 16; ++i) { xp[i] = rbf(xp[i]); mx = fmaxf(mx, xp[i]); mn = fminf(mn, xp[i]); }
    mx = fmaxf(mx, __shfl_xor(mx, 32)); mn = fminf(mn, __shfl_xor(mn, 32));
    float ep[16], en[16], sp = 0.f, sn = 0.f;
#pragma unroll
    for (int i = 0; i < 16; ++i) { ep[i] = __expf(xp[i] - mx); sp += ep[i]; en[i] = __expf(mn - xp[i]); sn += en[i]; }
    sp += __shfl_xor(sp, 32); sn += __shfl_xor(sn, 32);
    const float ip = 1.f / sp, in_ = 1.f / sn;
    float den = 0.f;
    bf16x8 pb[4];
#pragma unroll
    for (int s = 0; s < 2; ++s) {
      u32x4 wpk, wnk;
#pragma unroll
      for (int j = 0; j < 4; ++j) {
        wpk[j] = pk2(ep[8 * s + 2 * j] * ip, ep[8 * s + 2 * j + 1] * ip);
        wnk[j] = pk2(en[8 * s + 2 * j] * in_, en[8 * s + 2 * j + 1] * in_);
        den += lo2f(wpk[j]) * zp[8 * s + 2 * j] + hi2f(wpk[j]) * zp[8 * s + 2 * j + 1];
        den += lo2f(wnk[j]) * zn[8 * s + 2 * j] + hi2f(wnk[j]) * zn[8 * s + 2 * j + 1];
      }
      pb[s] = __builtin_bit_cast(bf16x8, wpk);
      pb[2 + s] = __builtin_bit_cast(bf16x8, wnk);
    }
    den += __shfl_xor(den, 32);
    den = rbf(rbf(den) + 1e-6f);
    f32x16 num;
#pragma unroll
    for (int i = 0; i < 16; ++i) num[i] = 0.f;
#pragma unroll
    for (int s = 0; s < 4; ++s) num = __builtin_amdgcn_mfma_f32_32x32x16_bf16(ka[s], pb[s], num, 0, 0, 0);
#pragma unroll
    for (int g = 0; g < 4; ++g) {
      u32x2 v = {pk2(rbf(num[4 * g]) / den, rbf(num[4 * g + 1]) / den), pk2(rbf(num[4 * g + 2]) / den, rbf(num[4 * g + 3]) / den)};
      *reinterpret_cast<u32x2*>(o + (size_t)row * D_ + h * 32 + 8 * g + 4 * lh) = v;
    }
  }
}

#define B3_OFF(row, ch) ((((row) >> 3) * 2 + ((ch) >> 2)) * 512 + ((row) & 7) * 64 + ((((ch) & 3) ^ (((row) >> 2) & 3)) << 4))
#define K3_STAGE 24576
__global__ __launch_bounds__(512) void k_wo(const u16* __restrict__ x, const u16* __restrict__ wo, char* __restrict__ ws) {
  __shared__ __attribute__((aligned(16))) char smem[2 * K3_STAGE];
  const int tid = threadIdx.x, lane = tid & 63, wid = tid >> 6;
  const int xcd = blockIdx.x & 7, idx = blockIdx.x >> 3;
  const int mt = (xcd & 3) * 4 + (idx & 3), nt = (xcd >> 2) * 8 + (idx >> 2);
  const int row0 = mt * 128, col0 = nt * 64;
  const u16* o = reinterpret_cast<const u16*>(ws + WS_O);
  const int sr = tid >> 3, sch = tid & 7;
  const u16* pa0 = o + (size_t)(row0 + sr) * D_ + sch * 8;
  const u16* pa1 = pa0 + 64 * D_;
  const u16* pb = wo + (size_t)sr * D_ + col0 + sch * 8;
  u32x4 rg[4][3];
  auto gload = [&](int t) {
    rg[t & 3][0] = *reinterpret_cast<const u32x4*>(pa0 + t * 64);
    rg[t & 3][1] = *reinterpret_cast<const u32x4*>(pa1 + t * 64);
    rg[t & 3][2] = *reinterpret_cast<const u32x4*>(pb + (size_t)t * 64 * D_);
  };
  auto swrite = [&](int t, int buf) {
    char* base = smem + buf * K3_STAGE;
    const u32x4 ga0 = rg[t & 3][0], ga1 = rg[t & 3][1], gb0 = rg[t & 3][2];
    *reinterpret_cast<u32x4*>(base + A_OFF(sr, sch)) = ga0;
    *reinterpret_cast<u32x4*>(base + A_OFF(sr + 64, sch)) = ga1;
    *reinterpret_cast<u32x4*>(base + 16384 + B3_OFF(sr, sch)) = gb0;
  };
  const int wm = wid & 3, wn = wid >> 2;
  const int l15 = lane & 15, lg = lane >> 4, lq = (lane & 15) >> 2, lp = lane & 3;
  f32x4 acc[2][2];
#pragma unroll
  for (int i = 0; i < 2; ++i)
#pragma unroll
    for (int j = 0; j < 2; ++j) acc[i][j] = f32x4{0.f, 0.f, 0.f, 0.f};
  gload(0); gload(1); gload(2);
  swrite(0, 0);
  __syncthreads();
#pragma unroll
  for (int t = 0; t < 16; ++t) {
    if (t < 13) gload(t + 3);
    const char* base = smem + (t & 1) * K3_STAGE;
#pragma unroll
    for (int ks = 0; ks < 2; ++ks) {
      bf16x8 a[2], b[2];
#pragma unroll
      for (int mi = 0; mi < 2; ++mi) a[mi] = *reinterpret_cast<const bf16x8*>(base + A_OFF(wm * 32 + mi * 16 + l15, ks * 4 + lg));
#pragma unroll
      for (int ni = 0; ni < 2; ++ni) {
        const int ch = wn * 4 + ni * 2 + (lp >> 1);
        const int kr = ks * 32 + lg * 8 + lq;
        const s16x4 lo = __builtin_amdgcn_ds_read_tr16_b64_v4i16((lds_s16x4*)(base + 16384 + B3_OFF(kr, ch) + 8 * (lp & 1)));
        const s16x4 hi = __builtin_amdgcn_ds_read_tr16_b64_v4i16((lds_s16x4*)(base + 16384 + B3_OFF(kr + 4, ch) + 8 * (lp & 1)));
        b[ni] = __builtin_shufflevector(lo, hi, 0, 1, 2, 3, 4, 5, 6, 7);
      }
#pragma unroll
      for (int mi = 0; mi < 2; ++mi)
#pragma unroll
        for (int ni = 0; ni < 2; ++ni)
          acc[mi][ni] = __builtin_amdgcn_mfma_f32_16x16x32_bf16(b[ni], a[mi], acc[mi][ni], 0, 0, 0);
    }
    if (t < 15) swrite(t + 1, (t + 1) & 1);
    __syncthreads();
  }
  const float* mf = reinterpret_cast<const float*>(ws + WS_MODF);
  const float* wr1 = reinterpret_cast<const float*>(ws + WS_WR1);
  u16* x1 = reinterpret_cast<u16*>(ws + WS_X1);
  float* rs = reinterpret_cast<float*>(ws + WS_RS2);
#pragma unroll
  for (int mi = 0; mi < 2; ++mi) {
    const int m = row0 + wm * 32 + mi * 16 + l15;
    float s1 = 0.f, s2 = 0.f, s3 = 0.f;
#pragma unroll
    for (int ni = 0; ni < 2; ++ni) {
      const int n = col0 + wn * 32 + ni * 16 + lg * 4;
      const u32x2 xv = *reinterpret_cast<const u32x2*>(x + (size_t)m * D_ + n);
      const f32x4 g = *reinterpret_cast<const f32x4*>(mf + 2048 + n);
      const f32x4 w = *reinterpret_cast<const f32x4*>(wr1 + n);
      float v[4];
      v[0] = rbf(lo2f(xv[0]) + rbf(g[0] * rbf(acc[mi][ni][0])));
      v[1] = rbf(hi2f(xv[0]) + rbf(g[1] * rbf(acc[mi][ni][1])));
      v[2] = rbf(lo2f(xv[1]) + rbf(g[2] * rbf(acc[mi][ni][2])));
      v[3] = rbf(hi2f(xv[1]) + rbf(g[3] * rbf(acc[mi][ni][3])));
      u32x2 ov = {pk2(v[0], v[1]), pk2(v[2], v[3])};
      *reinterpret_cast<u32x2*>(x1 + (size_t)m * D_ + n) = ov;
#pragma unroll
      for (int j = 0; j < 4; ++j) { s1 += v[j]; s2 += v[j] * v[j]; s3 += v[j] * w[j]; }
    }
    s1 += __shfl_xor(s1, 16); s1 += __shfl_xor(s1, 32);
    s2 += __shfl_xor(s2, 16); s2 += __shfl_xor(s2, 32);
    s3 += __shfl_xor(s3, 16); s3 += __shfl_xor(s3, 32);
    if (lg == 0) { atomicAdd(rs + m, s1); atomicAdd(rs + 2048 + m, s2); atomicAdd(rs + 4096 + m, s3); }
  }
}

__device__ __forceinline__ void row_stats2(const float* rs, int row, float& m, float& rstd) {
  const float mean = rs[row] * (1.f / D_);
  float var = rs[2048 + row] * (1.f / D_) - mean * mean;
  var = fmaxf(var, 0.f);
  m = rbf(mean);
  rstd = 1.f / rbf(sqrtf(rbf(rbf(var) + 1e-5f)));
}

__device__ __forceinline__ void st_agent(float* p, float v) { __hip_atomic_store(p, v, __ATOMIC_RELAXED, __HIP_MEMORY_SCOPE_AGENT); }
__device__ __forceinline__ float ld_agent(const float* p) { return __hip_atomic_load(p, __ATOMIC_RELAXED, __HIP_MEMORY_SCOPE_AGENT); }
__device__ __forceinline__ void signal_counter(unsigned* cnt) {
  asm volatile("s_waitcnt vmcnt(0)" ::: "memory");
  __syncthreads();
  if (threadIdx.x == 0) __hip_atomic_fetch_add(cnt, 1u, __ATOMIC_RELAXED, __HIP_MEMORY_SCOPE_AGENT);
}
__device__ __forceinline__ void wait_counter(unsigned* cnt, unsigned target) {
  if (threadIdx.x == 0) {
    for (unsigned spins = 0; spins < (1u << 22); ++spins) {
      if (__hip_atomic_load(cnt, __ATOMIC_RELAXED, __HIP_MEMORY_SCOPE_AGENT) >= target) break;
      __builtin_amdgcn_s_sleep(1);
    }
  }
  __syncthreads();
}

__global__ __launch_bounds__(256) void k_moe(const u16* __restrict__ wg, const u16* __restrict__ wu, const u16* __restrict__ wd,
                                             char* __restrict__ ws, u16* __restrict__ out) {
  const int tid = threadIdx.x, lane = tid & 63, wid = tid >> 6;
  const float* rs = reinterpret_cast<const float*>(ws + WS_RS2);
  const float* mf = reinterpret_cast<const float*>(ws + WS_MODF);
  const u16* x1 = reinterpret_cast<const u16*>(ws + WS_X1);
  if (blockIdx.x < 256) {
    __shared__ float sv[4][2];
    __shared__ int si[4][2];
    __shared__ float hs[2][256];
    __shared__ float red[4][4][64];
    __shared__ int last;
    const int b = blockIdx.x;
    const int c8 = tid & 7, kr = tid >> 3;
    float* gup = reinterpret_cast<float*>(ws + WS_GUP);
    unsigned* cnt = reinterpret_cast<unsigned*>(ws + WS_CNT);
    const int cg2 = b & 15, ks2 = b >> 4;
    if (b < 224) {
      const int cg = b % 56, ks = b / 56;
      float ra[8], rb[8], rc[8];
#pragma unroll
      for (int i = 0; i < 8; ++i) { ra[i] = rs[i * 256 + tid]; rb[i] = rs[2048 + i * 256 + tid]; rc[i] = rs[4096 + i * 256 + tid]; }
      const float W1 = reinterpret_cast<const float*>(ws + WS_W12)[0], W2 = reinterpret_cast<const float*>(ws + WS_W12)[1];
      const int kk = ks * 256 + tid;
      const float ma = mf[4096 + kk], ms = mf[3072 + kk];
      u32x4 g[8], u[8];
#pragma unroll
      for (int i = 0; i < 8; ++i) {
        const size_t off = (size_t)(ks * 256 + i * 32 + kr) * F_ + cg * 64 + c8 * 8;
        g[i] = *reinterpret_cast<const u32x4*>(wg + off);
        u[i] = *reinterpret_cast<const u32x4*>(wu + off);
      }
      __builtin_amdgcn_sched_barrier(0);
      float v1 = -3.0e38f, v2 = -3.0e38f; int i1 = 0x7fffffff, i2 = 0x7fffffff;
#pragma unroll
      for (int i = 0; i < 8; ++i) {
        const int row = i * 256 + tid;
        const float mean = ra[i] * (1.f / D_);
        const float var = fmaxf(rb[i] * (1.f / D_) - mean * mean, 0.f);
        const float m = rbf(mean), rstd = 1.f / rbf(sqrtf(rbf(rbf(var) + 1e-5f)));
        const float r = rbf(rstd * (rc[i] - m * W1) + W2);
        const bool b1 = r > v1, b2 = r > v2;
        v2 = b1 ? v1 : (b2 ? r : v2); i2 = b1 ? i1 : (b2 ? row : i2);
        v1 = b1 ? r : v1; i1 = b1 ? row : i1;
      }
#pragma unroll
      for (int o = 32; o > 0; o >>= 1) {
        const float a1 = __shfl_xor(v1, o), a2 = __shfl_xor(v2, o);
        const int b1 = __shfl_xor(i1, o), b2 = __shfl_xor(i2, o);
        if (a1 > v1 || (a1 == v1 && b1 < i1)) {
          if (v1 > a2 || (v1 == a2 && i1 < b2)) { v2 = v1; i2 = i1; } else { v2 = a2; i2 = b2; }
          v1 = a1; i1 = b1;
        } else if (a1 > v2 || (a1 == v2 && b1 < i2)) { v2 = a1; i2 = b1; }
      }
      if (lane == 0) { sv[wid][0] = v1; sv[wid][1] = v2; si[wid][0] = i1; si[wid][1] = i2; }
      __syncthreads();
      v1 = sv[0][0]; v2 = sv[0][1]; i1 = si[0][0]; i2 = si[0][1];
#pragma unroll
      for (int q = 1; q < 4; ++q) {
        const float a1 = sv[q][0], a2 = sv[q][1]; const int b1 = si[q][0], b2 = si[q][1];
        if (a1 > v1 || (a1 == v1 && b1 < i1)) {
          if (v1 > a2 || (v1 == a2 && i1 < b2)) { v2 = v1; i2 = i1; } else { v2 = a2; i2 = b2; }
          v1 = a1; i1 = b1;
        } else if (a1 > v2 || (a1 == v2 && b1 < i2)) { v2 = a1; i2 = b1; }
      }
      if (b == 0 && tid == 0) {
        int* sel = reinterpret_cast<int*>(ws + WS_SEL);
        __hip_atomic_store(sel + 0, i1, __ATOMIC_RELAXED, __HIP_MEMORY_SCOPE_AGENT);
        __hip_atomic_store(sel + 1, i2, __ATOMIC_RELAXED, __HIP_MEMORY_SCOPE_AGENT);
        st_agent(reinterpret_cast<float*>(sel) + 2, v1);
        st_agent(reinterpret_cast<float*>(sel) + 3, v2);
      }
      {
        float m, rstd;
        row_stats2(rs, i1, m, rstd);
        hs[0][tid] = rbf((bf2f(x1[(size_t)i1 * D_ + kk]) - m) * rstd * rbf(1.f + ma) + ms);
        row_stats2(rs, i2, m, rstd);
        hs[1][tid] = rbf((bf2f(x1[(size_t)i2 * D_ + kk]) - m) * rstd * rbf(1.f + ma) + ms);
      }
      __syncthreads();
      float ag[2][8], au[2][8];
#pragma unroll
      for (int r = 0; r < 2; ++r)
#pragma unroll
        for (int j = 0; j < 8; ++j) { ag[r][j] = 0.f; au[r][j] = 0.f; }
#pragma unroll
      for (int i = 0; i < 8; ++i) {
        const float h0 = hs[0][i * 32 + kr], h1 = hs[1][i * 32 + kr];
#pragma unroll
        for (int j = 0; j < 4; ++j) {
          ag[0][2 * j] += h0 * lo2f(g[i][j]); ag[0][2 * j + 1] += h0 * hi2f(g[i][j]);
          ag[1][2 * j] += h1 * lo2f(g[i][j]); ag[1][2 * j + 1] += h1 * hi2f(g[i][j]);
          au[0][2 * j] += h0 * lo2f(u[i][j]); au[0][2 * j + 1] += h0 * hi2f(u[i][j]);
          au[1][2 * j] += h1 * lo2f(u[i][j]); au[1][2 * j + 1] += h1 * hi2f(u[i][j]);
        }
      }
#pragma unroll
      for (int r = 0; r < 2; ++r)
#pragma unroll
        for (int j = 0; j < 8; ++j) {
          float a = ag[r][j], c = au[r][j];
          a += __shfl_xor(a, 8); a += __shfl_xor(a, 16); a += __shfl_xor(a, 32);
          c += __shfl_xor(c, 8); c += __shfl_xor(c, 16); c += __shfl_xor(c, 32);
          if (lane < 8) { red[wid][r][lane * 8 + j] = a; red[wid][2 + r][lane * 8 + j] = c; }
        }
      __syncthreads();
      {
        const int q = tid >> 6, cc = tid & 63;
        st_agent(gup + (size_t)(ks * 4 + q) * F_ + cg * 64 + cc, red[0][q][cc] + red[1][q][cc] + red[2][q][cc] + red[3][q][cc]);
      }
      signal_counter(cnt + 16);
    }
    u32x4 w[7];
#pragma unroll
    for (int i = 0; i < 7; ++i)
      w[i] = *reinterpret_cast<const u32x4*>(wd + (size_t)(ks2 * 224 + i * 32 + kr) * D_ + cg2 * 64 + c8 * 8);
    wait_counter(cnt + 16, 224u);
    if (tid < 224) {
      const int f = ks2 * 224 + tid;
      float gg[2][4], uu[2][4];
#pragma unroll
      for (int r = 0; r < 2; ++r)
#pragma unroll
        for (int p = 0; p < 4; ++p) { gg[r][p] = ld_agent(gup + (size_t)(p * 4 + r) * F_ + f); uu[r][p] = ld_agent(gup + (size_t)(p * 4 + 2 + r) * F_ + f); }
#pragma unroll
      for (int r = 0; r < 2; ++r)
        hs[r][tid] = rbf(rbf(silu_f(rbf(gg[r][0] + gg[r][1] + gg[r][2] + gg[r][3]))) * rbf(uu[r][0] + uu[r][1] + uu[r][2] + uu[r][3]));
    }
    __syncthreads();
    float a0[8], a1[8];
#pragma unroll
    for (int j = 0; j < 8; ++j) { a0[j] = 0.f; a1[j] = 0.f; }
#pragma unroll
    for (int i = 0; i < 7; ++i) {
      const float g0 = hs[0][i * 32 + kr], g1 = hs[1][i * 32 + kr];
#pragma unroll
      for (int j = 0; j < 4; ++j) {
        a0[2 * j] += g0 * lo2f(w[i][j]); a0[2 * j + 1] += g0 * hi2f(w[i][j]);
        a1[2 * j] += g1 * lo2f(w[i][j]); a1[2 * j + 1] += g1 * hi2f(w[i][j]);
      }
    }
#pragma unroll
    for (int j = 0; j < 8; ++j) {
      float a = a0[j], c = a1[j];
      a += __shfl_xor(a, 8); a += __shfl_xor(a, 16); a += __shfl_xor(a, 32);
      c += __shfl_xor(c, 8); c += __shfl_xor(c, 16); c += __shfl_xor(c, 32);
      if (lane < 8) { red[wid][0][lane * 8 + j] = a; red[wid][1][lane * 8 + j] = c; }
    }
    __syncthreads();
    float* yp = reinterpret_cast<float*>(ws + WS_YP);
    if (tid < 128) {
      const int r = tid >> 6, cc = tid & 63;
      atomicAdd(yp + (size_t)r * D_ + cg2 * 64 + cc, red[0][r][cc] + red[1][r][cc] + red[2][r][cc] + red[3][r][cc]);
    }
    asm volatile("s_waitcnt vmcnt(0)" ::: "memory");
    __syncthreads();
    if (tid == 0) {
      const unsigned t = __hip_atomic_fetch_add(cnt + cg2, 1u, __ATOMIC_RELAXED, __HIP_MEMORY_SCOPE_AGENT);
      last = (t == 15u);
    }
    __syncthreads();
    if (last && tid < 128) {
      const int r = tid >> 6, cc = tid & 63, n = cg2 * 64 + cc;
      float y = ld_agent(yp + (size_t)r * D_ + n);
      const int* sel = reinterpret_cast<const int*>(ws + WS_SEL);
      const int row = __hip_atomic_load(sel + r, __ATOMIC_RELAXED, __HIP_MEMORY_SCOPE_AGENT);
      const float rv = ld_agent(reinterpret_cast<const float*>(sel) + 2 + r);
      const float sg = rbf(1.f / (1.f + __expf(-rv)));
      y = rbf(rbf(y) * sg);
      float m, rstd; row_stats2(rs, row, m, rstd);
      const float xv = bf2f(x1[(size_t)row * D_ + n]);
      const float h2 = rbf((xv - m) * rstd * rbf(1.f + mf[4096 + n]) + mf[3072 + n]);
      const float B = rbf(h2 + y);
      out[(size_t)row * D_ + n] = (u16)(pk2(xv + rbf(mf[5120 + n] * B), 0.f) & 0xffff);
    }
  } else {
    __shared__ float sv[4][2];
    __shared__ int si[4][2];
    float v1 = -3.0e38f, v2 = -3.0e38f; int i1 = 0x7fffffff, i2 = 0x7fffffff;
    {
      float ra[8], rb[8], rc[8];
#pragma unroll
      for (int i = 0; i < 8; ++i) { ra[i] = rs[i * 256 + tid]; rb[i] = rs[2048 + i * 256 + tid]; rc[i] = rs[4096 + i * 256 + tid]; }
      const float W1 = reinterpret_cast<const float*>(ws + WS_W12)[0], W2 = reinterpret_cast<const float*>(ws + WS_W12)[1];
#pragma unroll
      for (int i = 0; i < 8; ++i) {
        const int row = i * 256 + tid;
        const float mean = ra[i] * (1.f / D_);
        const float var = fmaxf(rb[i] * (1.f / D_) - mean * mean, 0.f);
        const float m = rbf(mean), rstd = 1.f / rbf(sqrtf(rbf(rbf(var) + 1e-5f)));
        const float r = rbf(rstd * (rc[i] - m * W1) + W2);
        const bool b1 = r > v1, b2 = r > v2;
        v2 = b1 ? v1 : (b2 ? r : v2); i2 = b1 ? i1 : (b2 ? row : i2);
        v1 = b1 ? r : v1; i1 = b1 ? row : i1;
      }
#pragma unroll
      for (int o = 32; o > 0; o >>= 1) {
        const float a1 = __shfl_xor(v1, o), a2 = __shfl_xor(v2, o);
        const int b1 = __shfl_xor(i1, o), b2 = __shfl_xor(i2, o);
        if (a1 > v1 || (a1 == v1 && b1 < i1)) {
          if (v1 > a2 || (v1 == a2 && i1 < b2)) { v2 = v1; i2 = i1; } else { v2 = a2; i2 = b2; }
          v1 = a1; i1 = b1;
        } else if (a1 > v2 || (a1 == v2 && b1 < i2)) { v2 = a1; i2 = b1; }
      }
      if (lane == 0) { sv[wid][0] = v1; sv[wid][1] = v2; si[wid][0] = i1; si[wid][1] = i2; }
      __syncthreads();
      v1 = sv[0][0]; v2 = sv[0][1]; i1 = si[0][0]; i2 = si[0][1];
#pragma unroll
      for (int q = 1; q < 4; ++q) {
        const float a1 = sv[q][0], a2 = sv[q][1]; const int b1 = si[q][0], b2 = si[q][1];
        if (a1 > v1 || (a1 == v1 && b1 < i1)) {
          if (v1 > a2 || (v1 == a2 && i1 < b2)) { v2 = v1; i2 = i1; } else { v2 = a2; i2 = b2; }
          v1 = a1; i1 = b1;
        } else if (a1 > v2 || (a1 == v2 && b1 < i2)) { v2 = a1; i2 = b1; }
      }
    }
    const int rbase = (blockIdx.x - 256) * 8;
#pragma unroll 1
    for (int rr = wid; rr < 8; rr += 4) {
      const int row = rbase + rr;
      if (row == i1 || row == i2) continue;
      float m, rstd; row_stats2(rs, row, m, rstd);
#pragma unroll
      for (int half = 0; half < 2; ++half) {
        const int k = half * 512 + lane * 8;
        const u32x4 xv = *reinterpret_cast<const u32x4*>(x1 + (size_t)row * D_ + k);
        float a[8], sb[8], gb[8];
        *reinterpret_cast<f32x4*>(a) = *reinterpret_cast<const f32x4*>(mf + 4096 + k);
        *reinterpret_cast<f32x4*>(a + 4) = *reinterpret_cast<const f32x4*>(mf + 4096 + k + 4);
        *reinterpret_cast<f32x4*>(sb) = *reinterpret_cast<const f32x4*>(mf + 3072 + k);
        *reinterpret_cast<f32x4*>(sb + 4) = *reinterpret_cast<const f32x4*>(mf + 3072 + k + 4);
        *reinterpret_cast<f32x4*>(gb) = *reinterpret_cast<const f32x4*>(mf + 5120 + k);
        *reinterpret_cast<f32x4*>(gb + 4) = *reinterpret_cast<const f32x4*>(mf + 5120 + k + 4);
        u32x4 ov;
#pragma unroll
        for (int j = 0; j < 4; ++j) {
          const float xa = lo2f(xv[j]), xb = hi2f(xv[j]);
          const float ha = rbf((xa - m) * rstd * rbf(1.f + a[2 * j]) + sb[2 * j]);
          const float hb = rbf((xb - m) * rstd * rbf(1.f + a[2 * j + 1]) + sb[2 * j + 1]);
          ov[j] = pk2(xa + rbf(gb[2 * j] * ha), xb + rbf(gb[2 * j + 1] * hb));
        }
        *reinterpret_cast<u32x4*>(out + (size_t)row * D_ + k) = ov;
      }
    }
  }
}

extern "C" void kernel_launch(void* const* d_in, const int* in_sizes, int n_in, void* d_out, int out_size,
                              void* d_ws, size_t ws_size, hipStream_t stream) {
  const u16* x = (const u16*)d_in[0];
  const u16* wqkv = (const u16*)d_in[1];
  const u16* wphi = (const u16*)d_in[2];
  const u16* wo = (const u16*)d_in[3];
  const u16* wr = (const u16*)d_in[5];
  const u16* wg = (const u16*)d_in[6];
  const u16* wu = (const u16*)d_in[7];
  const u16* wd = (const u16*)d_in[8];
  const u16* c = (const u16*)d_in[9];
  const u16* wmod = (const u16*)d_in[10];
  char* ws = (char*)d_ws;
  u16* out = (u16*)d_out;
  hipFuncSetAttribute((const void*)k_qkv, hipFuncAttributeMaxDynamicSharedMemorySize, K1_LDS);
  k_prep<<<896, 256, 0, stream>>>(x, c, wmod, ws);
  k_qkv<<<256, 512, K1_LDS, stream>>>(x, wqkv, wphi, ws);
  k_o<<<257, 256, 0, stream>>>(wphi, wr, ws);
  k_wo<<<256, 512, 0, stream>>>(x, wo, ws);
  k_moe<<<512, 256, 0, stream>>>(wg, wu, wd, ws, out);
}
```

```cpp
#include <hip/hip_runtime.h>
#include <stdint.h>

typedef unsigned short u16;
typedef short bf16x8 __attribute__((ext_vector_type(8)));
typedef short s16x4 __attribute__((ext_vector_type(4)));
typedef float f32x2 __attribute__((ext_vector_type(2)));
typedef float f32x4 __attribute__((ext_vector_type(4)));
typedef float f32x16 __attribute__((ext_vector_type(16)));
typedef unsigned u32x4 __attribute__((ext_vector_type(4)));
typedef unsigned u32x2 __attribute__((ext_vector_type(2)));
typedef __bf16 bf16x2v __attribute__((ext_vector_type(2)));
typedef __attribute__((address_space(3))) s16x4 lds_s16x4;

#define S_ 2048
#define D_ 1024
#define H_ 32
#define F_ 3584

#define WS_MODP   0u
#define WS_STATS  (WS_MODP + 4u*6144u*4u)
#define WS_RS2    (WS_STATS + 2048u*8u)
#define WS_CNT    (WS_RS2 + 3u*2048u*4u)
#define WS_MODF   (WS_CNT + 256u)
#define WS_WR1    (WS_MODF + 6144u*4u)
#define WS_W12    (WS_WR1 + 1024u*4u)
#define WS_SEL    (WS_W12 + 64u)
#define WS_GUP    (WS_SEL + 64u)
#define WS_YP     (WS_GUP + 4u*2u*2u*3584u*4u)
#define WS_Q      (WS_YP + 2u*1024u*4u)
#define WS_O      (WS_Q + 2048u*1024u*2u)
#define WS_X1     (WS_O + 2048u*1024u*2u)
#define WS_KVP    (WS_X1 + 2048u*1024u*2u)
#define WS_WQT    (WS_KVP + 16u*32u*2112u*4u)
#define WS_WOT    (WS_WQT + 1024u*3072u*2u)
#define WS_END    (WS_WOT + 1024u*1024u*2u)

__device__ __forceinline__ float bf2f(u16 u) { return __uint_as_float(((unsigned)u) << 16); }
__device__ __forceinline__ unsigned pk2(float a, float b) {
  f32x2 f = {a, b}; bf16x2v h = __builtin_convertvector(f, bf16x2v); return __builtin_bit_cast(unsigned, h);
}
__device__ __forceinline__ float rbf(float a) {
  return __uint_as_float(pk2(a, 0.f) << 16);
}
__device__ __forceinline__ float lo2f(unsigned w) { return __uint_as_float(w << 16); }
__device__ __forceinline__ float hi2f(unsigned w) { return __uint_as_float(w & 0xffff0000u); }
__device__ __forceinline__ float wave_sum(float v) {
  for (int o = 32; o > 0; o >>= 1) v += __shfl_xor(v, o);
  return v;
}
__device__ __forceinline__ float silu_f(float x) { return x / (1.f + __expf(-x)); }

__global__ __launch_bounds__(256) void k_prep(const u16* __restrict__ x, const u16* __restrict__ c,
                                              const u16* __restrict__ wmod, const u16* __restrict__ wqkv,
                                              const u16* __restrict__ wo, char* __restrict__ ws) {
  const int tid = threadIdx.x, lane = tid & 63, wid = tid >> 6;
  if (blockIdx.x >= 896) {
    __shared__ __attribute__((aligned(16))) u16 tl[64 * 72];
    const int j = blockIdx.x - 896;
    const u16* W; int N, kb, nb; u16* dst;
    if (j < 768) { W = wqkv; N = 3072; kb = j & 15; nb = j >> 4; dst = reinterpret_cast<u16*>(ws + WS_WQT); }
    else { W = wo; N = 1024; kb = (j - 768) & 15; nb = (j - 768) >> 4; dst = reinterpret_cast<u16*>(ws + WS_WOT); }
    const int r = tid >> 2, cpart = (tid & 3) * 16;
    const u32x4 v0 = *reinterpret_cast<const u32x4*>(W + (size_t)(kb * 64 + r) * N + nb * 64 + cpart);
    const u32x4 v1 = *reinterpret_cast<const u32x4*>(W + (size_t)(kb * 64 + r) * N + nb * 64 + cpart + 8);
    *reinterpret_cast<u32x4*>(tl + r * 72 + cpart) = v0;
    *reinterpret_cast<u32x4*>(tl + r * 72 + cpart + 8) = v1;
    __syncthreads();
    const int lg = lane >> 4, lq = (lane & 15) >> 2, lp = lane & 3;
#pragma unroll
    for (int f = 0; f < 2; ++f) {
      const int fr = wid * 2 + f;
      const int ks = fr >> 2, nt = fr & 3;
      const u16* p = tl + (ks * 32 + lg * 8 + lq) * 72 + nt * 16 + lp * 4;
      const s16x4 lo = __builtin_amdgcn_ds_read_tr16_b64_v4i16((lds_s16x4*)p);
      const s16x4 hi = __builtin_amdgcn_ds_read_tr16_b64_v4i16((lds_s16x4*)(p + 4 * 72));
      const bf16x8 v = __builtin_shufflevector(lo, hi, 0, 1, 2, 3, 4, 5, 6, 7);
      *reinterpret_cast<bf16x8*>(dst + ((size_t)((nb * 4 + nt) * 32 + kb * 2 + ks) * 64 + lane) * 8) = v;
    }
    return;
  }
  if (blockIdx.x < 384) {
    __shared__ float red[4][64];
    const int cg = blockIdx.x % 96, ks = blockIdx.x / 96;
    const int c8 = tid & 7, kr = tid >> 3;
    u32x4 w[8];
    float sc[8];
#pragma unroll
    for (int i = 0; i < 8; ++i) {
      const int k = ks * 256 + i * 32 + kr;
      w[i] = *reinterpret_cast<const u32x4*>(wmod + (size_t)k * 6144 + cg * 64 + c8 * 8);
      sc[i] = rbf(silu_f(bf2f(c[k])));
    }
    float acc[8] = {0, 0, 0, 0, 0, 0, 0, 0};
#pragma unroll
    for (int i = 0; i < 8; ++i)
#pragma unroll
      for (int j = 0; j < 4; ++j) { acc[2 * j] += sc[i] * lo2f(w[i][j]); acc[2 * j + 1] += sc[i] * hi2f(w[i][j]); }
#pragma unroll
    for (int j = 0; j < 8; ++j) {
      float v = acc[j];
      v += __shfl_xor(v, 8); v += __shfl_xor(v, 16); v += __shfl_xor(v, 32);
      acc[j] = v;
    }
    if (lane < 8)
#pragma unroll
      for (int j = 0; j < 8; ++j) red[wid][lane * 8 + j] = acc[j];
    __syncthreads();
    if (tid < 64) {
      float v = red[0][tid] + red[1][tid] + red[2][tid] + red[3][tid];
      reinterpret_cast<float*>(ws + WS_MODP)[ks * 6144 + cg * 64 + tid] = v;
    }
  } else {
    const int row = (blockIdx.x - 384) * 4 + wid;
    const u32x4 a = *reinterpret_cast<const u32x4*>(x + (size_t)row * D_ + lane * 8);
    const u32x4 b = *reinterpret_cast<const u32x4*>(x + (size_t)row * D_ + 512 + lane * 8);
    float s = 0.f;
#pragma unroll
    for (int j = 0; j < 4; ++j) s += lo2f(a[j]) + hi2f(a[j]) + lo2f(b[j]) + hi2f(b[j]);
    const float mean = wave_sum(s) * (1.f / D_);
    float q = 0.f;
#pragma unroll
    for (int j = 0; j < 4; ++j) {
      float d;
      d = lo2f(a[j]) - mean; q += d * d; d = hi2f(a[j]) - mean; q += d * d;
      d = lo2f(b[j]) - mean; q += d * d; d = hi2f(b[j]) - mean; q += d * d;
    }
    const float var = wave_sum(q) * (1.f / D_);
    if (lane == 0) {
      const float sd = rbf(sqrtf(rbf(rbf(var) + 1e-5f)));
      float* st = reinterpret_cast<float*>(ws + WS_STATS);
      st[row * 2] = rbf(mean);
      st[row * 2 + 1] = 1.f / sd;
      float* rs = reinterpret_cast<float*>(ws + WS_RS2);
      rs[row] = 0.f; rs[2048 + row] = 0.f; rs[4096 + row] = 0.f;
    }
    if (blockIdx.x == 384 && tid < 64) reinterpret_cast<unsigned*>(ws + WS_CNT)[tid] = 0u;
    {
      const int bi = blockIdx.x - 384;
      if (bi < 2) reinterpret_cast<f32x4*>(ws + WS_YP)[bi * 256 + tid] = f32x4{0.f, 0.f, 0.f, 0.f};
    }
  }
}

#define A_OFF(row, ch) ((row) * 128 + ((((ch) ^ ((row) & 7))) << 4))
#define B_OFF(row, ch) ((((row) >> 3) * 6 + ((ch) >> 2)) * 512 + ((row) & 7) * 64 + ((((ch) & 3) ^ (((row) >> 2) & 3)) << 4))
#define K1_STAGE 16384
#define K1_MOD (92160)
#define K1_WPHI (K1_MOD + 8192)
#define K1_LDS (K1_WPHI + 4096)
#define C_STRIDE 400
#define PHI_OFF (128 * C_STRIDE)
#define PHI_STRIDE 144

__global__ __launch_bounds__(512) void k_qkv(const u16* __restrict__ x, const u16* __restrict__ wqkv,
                                             const u16* __restrict__ wphi, char* __restrict__ ws) {
  extern __shared__ __attribute__((aligned(16))) char smem[];
  const int tid = threadIdx.x, lane = tid & 63, wid = tid >> 6;
  const int xcd = blockIdx.x & 7, idx = blockIdx.x >> 3;
  const int mt = (xcd & 1) * 8 + (idx & 7), hp = (xcd >> 1) * 4 + (idx >> 3);
  const int row0 = mt * 128;
  float* lmod = reinterpret_cast<float*>(smem + K1_MOD);
  const int sr = tid >> 3, sch = tid & 7;
  const float* st = reinterpret_cast<const float*>(ws + WS_STATS);
  const float m0 = st[(row0 + sr) * 2], r0 = st[(row0 + sr) * 2 + 1];
  const float m1 = st[(row0 + sr + 64) * 2], r1 = st[(row0 + sr + 64) * 2 + 1];
  const u16* xa0 = x + (size_t)(row0 + sr) * D_ + sch * 8;
  const u16* xa1 = xa0 + 64 * D_;
  const int wm = wid & 1, wn = wid >> 1;
  const int l15 = lane & 15, lg = lane >> 4, lq = (lane & 15) >> 2, lp = lane & 3;
  const u16* wf[3];
#pragma unroll
  for (int ni = 0; ni < 3; ++ni) {
    const int nl = wn * 3 + ni;
    const int nt = (nl >> 2) * 64 + hp * 4 + (nl & 3);
    wf[ni] = reinterpret_cast<const u16*>(ws + WS_WQT) + ((size_t)nt * 32 * 64 + lane) * 8;
  }
  u32x4 rg[4][2];
  bf16x8 fb[3][6];
  auto gload = [&](int t) {
    rg[t & 3][0] = *reinterpret_cast<const u32x4*>(xa0 + t * 64);
    rg[t & 3][1] = *reinterpret_cast<const u32x4*>(xa1 + t * 64);
  };
  auto bload = [&](int t) {
#pragma unroll
    for (int ks = 0; ks < 2; ++ks)
#pragma unroll
      for (int ni = 0; ni < 3; ++ni) fb[t % 3][ks * 3 + ni] = *reinterpret_cast<const bf16x8*>(wf[ni] + (size_t)(t * 2 + ks) * 512);
  };
  auto swrite = [&](int t, int buf) {
    char* base = smem + buf * K1_STAGE;
    const u32x4 ga0 = rg[t & 3][0], ga1 = rg[t & 3][1];
    const f32x4 a0 = *reinterpret_cast<const f32x4*>(lmod + t * 64 + sch * 8);
    const f32x4 a1 = *reinterpret_cast<const f32x4*>(lmod + t * 64 + sch * 8 + 4);
    const f32x4 s0 = *reinterpret_cast<const f32x4*>(lmod + 1024 + t * 64 + sch * 8);
    const f32x4 s1 = *reinterpret_cast<const f32x4*>(lmod + 1024 + t * 64 + sch * 8 + 4);
    const float av[8] = {a0[0], a0[1], a0[2], a0[3], a1[0], a1[1], a1[2], a1[3]};
    const float sv[8] = {s0[0], s0[1], s0[2], s0[3], s1[0], s1[1], s1[2], s1[3]};
    u32x4 h0, h1;
#pragma unroll
    for (int j = 0; j < 4; ++j) {
      h0[j] = pk2((lo2f(ga0[j]) - m0) * r0 * av[2 * j] + sv[2 * j], (hi2f(ga0[j]) - m0) * r0 * av[2 * j + 1] + sv[2 * j + 1]);
      h1[j] = pk2((lo2f(ga1[j]) - m1) * r1 * av[2 * j] + sv[2 * j], (hi2f(ga1[j]) - m1) * r1 * av[2 * j + 1] + sv[2 * j + 1]);
    }
    *reinterpret_cast<u32x4*>(base + A_OFF(sr, sch)) = h0;
    *reinterpret_cast<u32x4*>(base + A_OFF(sr + 64, sch)) = h1;
  };
  f32x4 acc[4][3];
#pragma unroll
  for (int i = 0; i < 4; ++i)
#pragma unroll
    for (int j = 0; j < 3; ++j) acc[i][j] = f32x4{0.f, 0.f, 0.f, 0.f};

  gload(0); gload(1); gload(2);
  bload(0); bload(1);
  {
    if (tid < 256) *reinterpret_cast<u32x4*>(smem + K1_WPHI + tid * 16) = *reinterpret_cast<const u32x4*>(wphi + (size_t)hp * 2048 + tid * 8);
    const float* mp = reinterpret_cast<const float*>(ws + WS_MODP);
#pragma unroll
    for (int i = tid; i < 1024; i += 512) {
      const float sa = mp[i] + mp[6144 + i] + mp[2 * 6144 + i] + mp[3 * 6144 + i];
      const float ca = mp[1024 + i] + mp[6144 + 1024 + i] + mp[2 * 6144 + 1024 + i] + mp[3 * 6144 + 1024 + i];
      lmod[i] = rbf(1.f + rbf(ca));
      lmod[1024 + i] = rbf(sa);
    }
  }
  __syncthreads();
  swrite(0, 0);
  __syncthreads();
#pragma unroll
  for (int t = 0; t < 16; ++t) {
    if (t < 13) gload(t + 3);
    if (t < 14) bload(t + 2);
    const char* base = smem + (t & 1) * K1_STAGE;
#pragma unroll
    for (int ks = 0; ks < 2; ++ks) {
      bf16x8 a[4];
#pragma unroll
      for (int mi = 0; mi < 4; ++mi) a[mi] = *reinterpret_cast<const bf16x8*>(base + A_OFF(wm * 64 + mi * 16 + l15, ks * 4 + lg));
#pragma unroll
      for (int mi = 0; mi < 4; ++mi)
#pragma unroll
        for (int ni = 0; ni < 3; ++ni)
          acc[mi][ni] = __builtin_amdgcn_mfma_f32_16x16x32_bf16(fb[t % 3][ks * 3 + ni], a[mi], acc[mi][ni], 0, 0, 0);
    }
    if (t < 15) swrite(t + 1, (t + 1) & 1);
    __syncthreads();
  }
#pragma unroll
  for (int mi = 0; mi < 4; ++mi)
#pragma unroll
    for (int ni = 0; ni < 3; ++ni) {
      const int m = wm * 64 + mi * 16 + l15, n = wn * 48 + ni * 16 + lg * 4;
      u32x2 v = {pk2(acc[mi][ni][0], acc[mi][ni][1]), pk2(acc[mi][ni][2], acc[mi][ni][3])};
      *reinterpret_cast<u32x2*>(smem + m * C_STRIDE + n * 2) = v;
    }
  __syncthreads();
  {
    u16* q = reinterpret_cast<u16*>(ws + WS_Q);
#pragma unroll
    for (int i = 0; i < 2; ++i) {
      const int r = sr + i * 64;
      const u32x4 v = *reinterpret_cast<const u32x4*>(smem + r * C_STRIDE + sch * 16);
      *reinterpret_cast<u32x4*>(q + (size_t)(row0 + r) * D_ + hp * 64 + sch * 8) = v;
    }
  }
  const int l31 = lane & 31, lh = lane >> 5;
  {
    const int hh = wid & 1, rt = wid >> 1;
    const u16* wp = reinterpret_cast<const u16*>(smem + K1_WPHI) + hh * 1024;
    f32x16 xp;
#pragma unroll
    for (int i = 0; i < 16; ++i) xp[i] = 0.f;
#pragma unroll
    for (int s = 0; s < 2; ++s) {
      bf16x8 a;
#pragma unroll
      for (int j = 0; j < 8; ++j) a[j] = (short)wp[(16 * s + 8 * lh + j) * 32 + l31];
      const bf16x8 b = *reinterpret_cast<const bf16x8*>(smem + (rt * 32 + l31) * C_STRIDE + (64 + hh * 32 + 16 * s + 8 * lh) * 2);
      xp = __builtin_amdgcn_mfma_f32_32x32x16_bf16(a, b, xp, 0, 0, 0);
    }
    float mx = -3.0e38f, mn = 3.0e38f;
#pragma unroll
    for (int i = 0; i < 16; ++i) { xp[i] = rbf(xp[i]); mx = fmaxf(mx, xp[i]); mn = fminf(mn, xp[i]); }
    mx = fmaxf(mx, __shfl_xor(mx, 32)); mn = fminf(mn, __shfl_xor(mn, 32));
    float ep[16], en[16], sp = 0.f, sn = 0.f;
#pragma unroll
    for (int i = 0; i < 16; ++i) { ep[i] = __expf(xp[i] - mx); sp += ep[i]; en[i] = __expf(mn - xp[i]); sn += en[i]; }
    sp += __shfl_xor(sp, 32); sn += __shfl_xor(sn, 32);
    const float ip = 1.f / sp, in_ = 1.f / sn;
    char* ph = smem + PHI_OFF + (hh * 128 + rt * 32 + l31) * PHI_STRIDE;
#pragma unroll
    for (int g = 0; g < 4; ++g) {
      u32x2 v = {pk2(ep[4 * g] * ip, ep[4 * g + 1] * ip), pk2(ep[4 * g + 2] * ip, ep[4 * g + 3] * ip)};
      *reinterpret_cast<u32x2*>(ph + (8 * g + 4 * lh) * 2) = v;
      u32x2 w = {pk2(en[4 * g] * in_, en[4 * g + 1] * in_), pk2(en[4 * g + 2] * in_, en[4 * g + 3] * in_)};
      *reinterpret_cast<u32x2*>(ph + 64 + (8 * g + 4 * lh) * 2) = w;
    }
  }
  __syncthreads();
  if (wid < 4) {
    const int hh = wid & 1, ft = wid >> 1;
    const int blk = (lane >> 4) & 1;
    f32x16 kv, zz;
#pragma unroll
    for (int i = 0; i < 16; ++i) { kv[i] = 0.f; zz[i] = 0.f; }
    bf16x8 ones;
#pragma unroll
    for (int j = 0; j < 8; ++j) ones[j] = (short)0x3f80;
#pragma unroll
    for (int s = 0; s < 8; ++s) {
      const char* pa = smem + PHI_OFF + (hh * 128 + 16 * s + 8 * lh + lq) * PHI_STRIDE + (ft * 32 + 16 * blk + 4 * lp) * 2;
      const s16x4 alo = __builtin_amdgcn_ds_read_tr16_b64_v4i16((lds_s16x4*)pa);
      const s16x4 ahi = __builtin_amdgcn_ds_read_tr16_b64_v4i16((lds_s16x4*)(pa + 4 * PHI_STRIDE));
      const bf16x8 a = __builtin_shufflevector(alo, ahi, 0, 1, 2, 3, 4, 5, 6, 7);
      const char* pb = smem + (16 * s + 8 * lh + lq) * C_STRIDE + (128 + hh * 32 + 16 * blk + 4 * lp) * 2;
      const s16x4 blo = __builtin_amdgcn_ds_read_tr16_b64_v4i16((lds_s16x4*)pb);
      const s16x4 bhi = __builtin_amdgcn_ds_read_tr16_b64_v4i16((lds_s16x4*)(pb + 4 * C_STRIDE));
      const bf16x8 b = __builtin_shufflevector(blo, bhi, 0, 1, 2, 3, 4, 5, 6, 7);
      kv = __builtin_amdgcn_mfma_f32_32x32x16_bf16(a, b, kv, 0, 0, 0);
      zz = __builtin_amdgcn_mfma_f32_32x32x16_bf16(a, ones, zz, 0, 0, 0);
    }
    float* kp = reinterpret_cast<float*>(ws + WS_KVP) + (size_t)((hp * 2 + hh) * 16 + mt) * 2112;
#pragma unroll
    for (int i = 0; i < 16; ++i) {
      const int f = ft * 32 + (i & 3) + 8 * (i >> 2) + 4 * lh;
      kp[f * 32 + l31] = kv[i];
      if (l31 == 0) kp[2048 + f] = zz[i];
    }
  }
}

__global__ __launch_bounds__(256) void k_o(const u16* __restrict__ wphi, const u16* __restrict__ wr, char* __restrict__ ws) {
  __shared__ __attribute__((aligned(16))) u16 kvt[32 * 68 + 64];
  __shared__ float zs[64];
  __shared__ float red[8];
  const int tid = threadIdx.x, lane = tid & 63, wid = tid >> 6;
  if (blockIdx.x == 256) {
    const float* mp = reinterpret_cast<const float*>(ws + WS_MODP);
    float* mf = reinterpret_cast<float*>(ws + WS_MODF);
    float* wr1 = reinterpret_cast<float*>(ws + WS_WR1);
    float s1 = 0.f, s2 = 0.f;
#pragma unroll
    for (int i = tid; i < 1024; i += 256) {
#pragma unroll
      for (int q = 0; q < 6; ++q)
        mf[q * 1024 + i] = rbf(mp[q * 1024 + i] + mp[6144 + q * 1024 + i] + mp[2 * 6144 + q * 1024 + i] + mp[3 * 6144 + q * 1024 + i]);
      const float sb = rbf(mp[3072 + i] + mp[6144 + 3072 + i] + mp[2 * 6144 + 3072 + i] + mp[3 * 6144 + 3072 + i]);
      const float cb = rbf(mp[4096 + i] + mp[6144 + 4096 + i] + mp[2 * 6144 + 4096 + i] + mp[3 * 6144 + 4096 + i]);
      const float w = bf2f(wr[i]);
      const float a = rbf(1.f + cb) * w;
      wr1[i] = a; s1 += a; s2 += sb * w;
    }
    s1 = wave_sum(s1); s2 = wave_sum(s2);
    if (lane == 0) { red[wid] = s1; red[4 + wid] = s2; }
    __syncthreads();
    if (tid == 0) {
      float* w12 = reinterpret_cast<float*>(ws + WS_W12);
      w12[0] = red[0] + red[1] + red[2] + red[3];
      w12[1] = red[4] + red[5] + red[6] + red[7];
    }
    return;
  }
  const int h = blockIdx.x & 31, rc = blockIdx.x >> 5;
  const int l31 = lane & 31, lh = lane >> 5;
  const u16* q = reinterpret_cast<const u16*>(ws + WS_Q);
  bf16x8 qb[2][2];
#pragma unroll
  for (int rt = 0; rt < 2; ++rt)
#pragma unroll
    for (int s = 0; s < 2; ++s)
      qb[rt][s] = *reinterpret_cast<const bf16x8*>(q + (size_t)(rc * 256 + wid * 64 + rt * 32 + l31) * D_ + h * 32 + 16 * s + 8 * lh);
  const u16* wp = wphi + (size_t)h * 1024;
  bf16x8 wa[2];
#pragma unroll
  for (int s = 0; s < 2; ++s)
#pragma unroll
    for (int j = 0; j < 8; ++j) wa[s][j] = (short)wp[(16 * s + 8 * lh + j) * 32 + l31];
  {
    const f32x4* kp = reinterpret_cast<const f32x4*>(ws + WS_KVP) + (size_t)h * 16 * 528;
    f32x4 s0 = {0.f, 0.f, 0.f, 0.f}, s1 = s0, s2 = s0;
#pragma unroll
    for (int m = 0; m < 16; ++m) {
      s0 += kp[m * 528 + tid];
      s1 += kp[m * 528 + 256 + tid];
    }
    if (wid == 0) {
#pragma unroll
      for (int m = 0; m < 16; ++m) s2 += kp[m * 528 + 512 + (lane & 15)];
    }
    {
      const int f = tid >> 3, d = (tid & 7) * 4;
#pragma unroll
      for (int j = 0; j < 4; ++j) {
        kvt[(d + j) * 68 + f] = (u16)(pk2(s0[j], 0.f) & 0xffff);
        kvt[(d + j) * 68 + 32 + f] = (u16)(pk2(s1[j], 0.f) & 0xffff);
      }
    }
    if (tid < 16)
#pragma unroll
      for (int j = 0; j < 4; ++j) zs[tid * 4 + j] = rbf(s2[j]);
  }
  __syncthreads();
  bf16x8 ka[4];
#pragma unroll
  for (int s = 0; s < 4; ++s) {
    const s16x4 lo = *reinterpret_cast<const s16x4*>(kvt + l31 * 68 + (s >> 1) * 32 + 16 * (s & 1) + 4 * lh);
    const s16x4 hi = *reinterpret_cast<const s16x4*>(kvt + l31 * 68 + (s >> 1) * 32 + 16 * (s & 1) + 8 + 4 * lh);
    ka[s] = __builtin_shufflevector(lo, hi, 0, 1, 2, 3, 4, 5, 6, 7);
  }
  float zp[16], zn[16];
#pragma unroll
  for (int i = 0; i < 16; ++i) { const int f = (i & 3) + 8 * (i >> 2) + 4 * lh; zp[i] = zs[f]; zn[i] = zs[32 + f]; }
  u16* o = reinterpret_cast<u16*>(ws + WS_O);
#pragma unroll
  for (int rt = 0; rt < 2; ++rt) {
    const int row = rc * 256 + wid * 64 + rt * 32 + l31;
    f32x16 xp;
#pragma unroll
    for (int i = 0; i < 16; ++i) xp[i] = 0.f;
#pragma unroll
    for (int s = 0; s < 2; ++s) {
      xp = __builtin_amdgcn_mfma_f32_32x32x16_bf16(wa[s], qb[rt][s], xp, 0, 0, 0);
    }
    float mx = -3.0e38f, mn = 3.0e38f;
#pragma unroll
    for (int i = 0; i < 16; ++i) { xp[i] = rbf(xp[i]); mx = fmaxf(mx, xp[i]); mn = fminf(mn, xp[i]); }
    mx = fmaxf(mx, __shfl_xor(mx, 32)); mn = fminf(mn, __shfl_xor(mn, 32));
    float ep[16], en[16], sp = 0.f, sn = 0.f;
#pragma unroll
    for (int i = 0; i < 16; ++i) { ep[i] = __expf(xp[i] - mx); sp += ep[i]; en[i] = __expf(mn - xp[i]); sn += en[i]; }
    sp += __shfl_xor(sp, 32); sn += __shfl_xor(sn, 32);
    const float ip = 1.f / sp, in_ = 1.f / sn;
    float den = 0.f;
    bf16x8 pb[4];
#pragma unroll
    for (int s = 0; s < 2; ++s) {
      u32x4 wpk, wnk;
#pragma unroll
      for (int j = 0; j < 4; ++j) {
        wpk[j] = pk2(ep[8 * s + 2 * j] * ip, ep[8 * s + 2 * j + 1] * ip);
        wnk[j] = pk2(en[8 * s + 2 * j] * in_, en[8 * s + 2 * j + 1] * in_);
        den += lo2f(wpk[j]) * zp[8 * s + 2 * j] + hi2f(wpk[j]) * zp[8 * s + 2 * j + 1];
        den += lo2f(wnk[j]) * zn[8 * s + 2 * j] + hi2f(wnk[j]) * zn[8 * s + 2 * j + 1];
      }
      pb[s] = __builtin_bit_cast(bf16x8, wpk);
      pb[2 + s] = __builtin_bit_cast(bf16x8, wnk);
    }
    den += __shfl_xor(den, 32);
    den = rbf(rbf(den) + 1e-6f);
    f32x16 num;
#pragma unroll
    for (int i = 0; i < 16; ++i) num[i] = 0.f;
#pragma unroll
    for (int s = 0; s < 4; ++s) num = __builtin_amdgcn_mfma_f32_32x32x16_bf16(ka[s], pb[s], num, 0, 0, 0);
#pragma unroll
    for (int g = 0; g < 4; ++g) {
      u32x2 v = {pk2(rbf(num[4 * g]) / den, rbf(num[4 * g + 1]) / den), pk2(rbf(num[4 * g + 2]) / den, rbf(num[4 * g + 3]) / den)};
      *reinterpret_cast<u32x2*>(o + (size_t)row * D_ + h * 32 + 8 * g + 4 * lh) = v;
    }
  }
}

#define B3_OFF(row, ch) ((((row) >> 3) * 2 + ((ch) >> 2)) * 512 + ((row) & 7) * 64 + ((((ch) & 3) ^ (((row) >> 2) & 3)) << 4))
#define K3_STAGE 24576
__global__ __launch_bounds__(512) void k_wo(const u16* __restrict__ x, const u16* __restrict__ wo, char* __restrict__ ws) {
  __shared__ __attribute__((aligned(16))) char smem[2 * K3_STAGE];
  const int tid = threadIdx.x, lane = tid & 63, wid = tid >> 6;
  const int xcd = blockIdx.x & 7, idx = blockIdx.x >> 3;
  const int mt = (xcd & 3) * 4 + (idx & 3), nt = (xcd >> 2) * 8 + (idx >> 2);
  const int row0 = mt * 128, col0 = nt * 64;
  const u16* o = reinterpret_cast<const u16*>(ws + WS_O);
  const int sr = tid >> 3, sch = tid & 7;
  const u16* pa0 = o + (size_t)(row0 + sr) * D_ + sch * 8;
  const u16* pa1 = pa0 + 64 * D_;
  const u16* pb = wo + (size_t)sr * D_ + col0 + sch * 8;
  u32x4 rg[4][3];
  auto gload = [&](int t) {
    rg[t & 3][0] = *reinterpret_cast<const u32x4*>(pa0 + t * 64);
    rg[t & 3][1] = *reinterpret_cast<const u32x4*>(pa1 + t * 64);
    rg[t & 3][2] = *reinterpret_cast<const u32x4*>(pb + (size_t)t * 64 * D_);
  };
  auto swrite = [&](int t, int buf) {
    char* base = smem + buf * K3_STAGE;
    const u32x4 ga0 = rg[t & 3][0], ga1 = rg[t & 3][1], gb0 = rg[t & 3][2];
    *reinterpret_cast<u32x4*>(base + A_OFF(sr, sch)) = ga0;
    *reinterpret_cast<u32x4*>(base + A_OFF(sr + 64, sch)) = ga1;
    *reinterpret_cast<u32x4*>(base + 16384 + B3_OFF(sr, sch)) = gb0;
  };
  const int wm = wid & 3, wn = wid >> 2;
  const int l15 = lane & 15, lg = lane >> 4, lq = (lane & 15) >> 2, lp = lane & 3;
  f32x4 acc[2][2];
#pragma unroll
  for (int i = 0; i < 2; ++i)
#pragma unroll
    for (int j = 0; j < 2; ++j) acc[i][j] = f32x4{0.f, 0.f, 0.f, 0.f};
  gload(0); gload(1); gload(2);
  swrite(0, 0);
  __syncthreads();
#pragma unroll
  for (int t = 0; t < 16; ++t) {
    if (t < 13) gload(t + 3);
    const char* base = smem + (t & 1) * K3_STAGE;
#pragma unroll
    for (int ks = 0; ks < 2; ++ks) {
      bf16x8 a[2], b[2];
#pragma unroll
      for (int mi = 0; mi < 2; ++mi) a[mi] = *reinterpret_cast<const bf16x8*>(base + A_OFF(wm * 32 + mi * 16 + l15, ks * 4 + lg));
#pragma unroll
      for (int ni = 0; ni < 2; ++ni) {
        const int ch = wn * 4 + ni * 2 + (lp >> 1);
        const int kr = ks * 32 + lg * 8 + lq;
        const s16x4 lo = __builtin_amdgcn_ds_read_tr16_b64_v4i16((lds_s16x4*)(base + 16384 + B3_OFF(kr, ch) + 8 * (lp & 1)));
        const s16x4 hi = __builtin_amdgcn_ds_read_tr16_b64_v4i16((lds_s16x4*)(base + 16384 + B3_OFF(kr + 4, ch) + 8 * (lp & 1)));
        b[ni] = __builtin_shufflevector(lo, hi, 0, 1, 2, 3, 4, 5, 6, 7);
      }
#pragma unroll
      for (int mi = 0; mi < 2; ++mi)
#pragma unroll
        for (int ni = 0; ni < 2; ++ni)
          acc[mi][ni] = __builtin_amdgcn_mfma_f32_16x16x32_bf16(b[ni], a[mi], acc[mi][ni], 0, 0, 0);
    }
    if (t < 15) swrite(t + 1, (t + 1) & 1);
    __syncthreads();
  }
  const float* mf = reinterpret_cast<const float*>(ws + WS_MODF);
  const float* wr1 = reinterpret_cast<const float*>(ws + WS_WR1);
  u16* x1 = reinterpret_cast<u16*>(ws + WS_X1);
  float* rs = reinterpret_cast<float*>(ws + WS_RS2);
#pragma unroll
  for (int mi = 0; mi < 2; ++mi) {
    const int m = row0 + wm * 32 + mi * 16 + l15;
    float s1 = 0.f, s2 = 0.f, s3 = 0.f;
#pragma unroll
    for (int ni = 0; ni < 2; ++ni) {
      const int n = col0 + wn * 32 + ni * 16 + lg * 4;
      const u32x2 xv = *reinterpret_cast<const u32x2*>(x + (size_t)m * D_ + n);
      const f32x4 g = *reinterpret_cast<const f32x4*>(mf + 2048 + n);
      const f32x4 w = *reinterpret_cast<const f32x4*>(wr1 + n);
      float v[4];
      v[0] = rbf(lo2f(xv[0]) + rbf(g[0] * rbf(acc[mi][ni][0])));
      v[1] = rbf(hi2f(xv[0]) + rbf(g[1] * rbf(acc[mi][ni][1])));
      v[2] = rbf(lo2f(xv[1]) + rbf(g[2] * rbf(acc[mi][ni][2])));
      v[3] = rbf(hi2f(xv[1]) + rbf(g[3] * rbf(acc[mi][ni][3])));
      u32x2 ov = {pk2(v[0], v[1]), pk2(v[2], v[3])};
      *reinterpret_cast<u32x2*>(x1 + (size_t)m * D_ + n) = ov;
#pragma unroll
      for (int j = 0; j < 4; ++j) { s1 += v[j]; s2 += v[j] * v[j]; s3 += v[j] * w[j]; }
    }
    s1 += __shfl_xor(s1, 16); s1 += __shfl_xor(s1, 32);
    s2 += __shfl_xor(s2, 16); s2 += __shfl_xor(s2, 32);
    s3 += __shfl_xor(s3, 16); s3 += __shfl_xor(s3, 32);
    if (lg == 0) { atomicAdd(rs + m, s1); atomicAdd(rs + 2048 + m, s2); atomicAdd(rs + 4096 + m, s3); }
  }
}

__device__ __forceinline__ void row_stats2(const float* rs, int row, float& m, float& rstd) {
  const float mean = rs[row] * (1.f / D_);
  float var = rs[2048 + row] * (1.f / D_) - mean * mean;
  var = fmaxf(var, 0.f);
  m = rbf(mean);
  rstd = 1.f / rbf(sqrtf(rbf(rbf(var) + 1e-5f)));
}

__global__ __launch_bounds__(256) void k_gateup(const u16* __restrict__ wg, const u16* __restrict__ wu,
                                                char* __restrict__ ws, u16* __restrict__ out) {
  const int tid = threadIdx.x, lane = tid & 63, wid = tid >> 6;
  const float* rs = reinterpret_cast<const float*>(ws + WS_RS2);
  const float* mf = reinterpret_cast<const float*>(ws + WS_MODF);
  const u16* x1 = reinterpret_cast<const u16*>(ws + WS_X1);
  if (blockIdx.x < 224) {
    __shared__ float sv[4][2];
    __shared__ int si[4][2];
    __shared__ float hs[2][256];
    __shared__ float red[4][4][64];
    const int cg = blockIdx.x % 56, ks = blockIdx.x / 56;
    const int c8 = tid & 7, kr = tid >> 3;
    float ra[8], rb[8], rc[8];
#pragma unroll
    for (int i = 0; i < 8; ++i) { ra[i] = rs[i * 256 + tid]; rb[i] = rs[2048 + i * 256 + tid]; rc[i] = rs[4096 + i * 256 + tid]; }
    const float W1 = reinterpret_cast<const float*>(ws + WS_W12)[0], W2 = reinterpret_cast<const float*>(ws + WS_W12)[1];
    const int kk = ks * 256 + tid;
    const float ma = mf[4096 + kk], ms = mf[3072 + kk];
    u32x4 g[8], u[8];
#pragma unroll
    for (int i = 0; i < 8; ++i) {
      const size_t off = (size_t)(ks * 256 + i * 32 + kr) * F_ + cg * 64 + c8 * 8;
      g[i] = *reinterpret_cast<const u32x4*>(wg + off);
      u[i] = *reinterpret_cast<const u32x4*>(wu + off);
    }
    __builtin_amdgcn_sched_barrier(0);
    float v1 = -3.0e38f, v2 = -3.0e38f; int i1 = 0x7fffffff, i2 = 0x7fffffff;
#pragma unroll
    for (int i = 0; i < 8; ++i) {
      const int row = i * 256 + tid;
      const float mean = ra[i] * (1.f / D_);
      const float var = fmaxf(rb[i] * (1.f / D_) - mean * mean, 0.f);
      const float m = rbf(mean), rstd = 1.f / rbf(sqrtf(rbf(rbf(var) + 1e-5f)));
      const float r = rbf(rstd * (rc[i] - m * W1) + W2);
      const bool b1 = r > v1, b2 = r > v2;
      v2 = b1 ? v1 : (b2 ? r : v2); i2 = b1 ? i1 : (b2 ? row : i2);
      v1 = b1 ? r : v1; i1 = b1 ? row : i1;
    }
#pragma unroll
    for (int o = 32; o > 0; o >>= 1) {
      const float a1 = __shfl_xor(v1, o), a2 = __shfl_xor(v2, o);
      const int b1 = __shfl_xor(i1, o), b2 = __shfl_xor(i2, o);
      if (a1 > v1 || (a1 == v1 && b1 < i1)) {
        if (v1 > a2 || (v1 == a2 && i1 < b2)) { v2 = v1; i2 = i1; } else { v2 = a2; i2 = b2; }
        v1 = a1; i1 = b1;
      } else if (a1 > v2 || (a1 == v2 && b1 < i2)) { v2 = a1; i2 = b1; }
    }
    if (lane == 0) { sv[wid][0] = v1; sv[wid][1] = v2; si[wid][0] = i1; si[wid][1] = i2; }
    __syncthreads();
    v1 = sv[0][0]; v2 = sv[0][1]; i1 = si[0][0]; i2 = si[0][1];
#pragma unroll
    for (int w = 1; w < 4; ++w) {
      const float a1 = sv[w][0], a2 = sv[w][1]; const int b1 = si[w][0], b2 = si[w][1];
      if (a1 > v1 || (a1 == v1 && b1 < i1)) {
        if (v1 > a2 || (v1 == a2 && i1 < b2)) { v2 = v1; i2 = i1; } else { v2 = a2; i2 = b2; }
        v1 = a1; i1 = b1;
      } else if (a1 > v2 || (a1 == v2 && b1 < i2)) { v2 = a1; i2 = b1; }
    }
    if (blockIdx.x == 0 && tid == 0) {
      int* sel = reinterpret_cast<int*>(ws + WS_SEL);
      sel[0] = i1; sel[1] = i2;
      reinterpret_cast<float*>(sel)[2] = v1; reinterpret_cast<float*>(sel)[3] = v2;
    }
    {
      const int k = kk;
      const float a = rbf(1.f + ma), s = ms;
      float m, rstd;
      row_stats2(rs, i1, m, rstd);
      hs[0][tid] = rbf((bf2f(x1[(size_t)i1 * D_ + k]) - m) * rstd * a + s);
      row_stats2(rs, i2, m, rstd);
      hs[1][tid] = rbf((bf2f(x1[(size_t)i2 * D_ + k]) - m) * rstd * a + s);
    }
    __syncthreads();
    float ag[2][8], au[2][8];
#pragma unroll
    for (int r = 0; r < 2; ++r)
#pragma unroll
      for (int j = 0; j < 8; ++j) { ag[r][j] = 0.f; au[r][j] = 0.f; }
#pragma unroll
    for (int i = 0; i < 8; ++i) {
      const float h0 = hs[0][i * 32 + kr], h1 = hs[1][i * 32 + kr];
#pragma unroll
      for (int j = 0; j < 4; ++j) {
        ag[0][2 * j] += h0 * lo2f(g[i][j]); ag[0][2 * j + 1] += h0 * hi2f(g[i][j]);
        ag[1][2 * j] += h1 * lo2f(g[i][j]); ag[1][2 * j + 1] += h1 * hi2f(g[i][j]);
        au[0][2 * j] += h0 * lo2f(u[i][j]); au[0][2 * j + 1] += h0 * hi2f(u[i][j]);
        au[1][2 * j] += h1 * lo2f(u[i][j]); au[1][2 * j + 1] += h1 * hi2f(u[i][j]);
      }
    }
#pragma unroll
    for (int r = 0; r < 2; ++r)
#pragma unroll
      for (int j = 0; j < 8; ++j) {
        float a = ag[r][j], b = au[r][j];
        a += __shfl_xor(a, 8); a += __shfl_xor(a, 16); a += __shfl_xor(a, 32);
        b += __shfl_xor(b, 8); b += __shfl_xor(b, 16); b += __shfl_xor(b, 32);
        if (lane < 8) { red[wid][r][lane * 8 + j] = a; red[wid][2 + r][lane * 8 + j] = b; }
      }
    __syncthreads();
    {
      const int q = tid >> 6, cc = tid & 63;
      const float v = red[0][q][cc] + red[1][q][cc] + red[2][q][cc] + red[3][q][cc];
      reinterpret_cast<float*>(ws + WS_GUP)[(size_t)(ks * 4 + q) * F_ + cg * 64 + cc] = v;
    }
  } else {
    const int rbase = (blockIdx.x - 224) * 8;
#pragma unroll 1
    for (int rr = wid; rr < 8; rr += 4) {
      const int row = rbase + rr;
      float m, rstd; row_stats2(rs, row, m, rstd);
#pragma unroll
      for (int half = 0; half < 2; ++half) {
        const int k = half * 512 + lane * 8;
        const u32x4 xv = *reinterpret_cast<const u32x4*>(x1 + (size_t)row * D_ + k);
        float a[8], s[8], gb[8];
        *reinterpret_cast<f32x4*>(a) = *reinterpret_cast<const f32x4*>(mf + 4096 + k);
        *reinterpret_cast<f32x4*>(a + 4) = *reinterpret_cast<const f32x4*>(mf + 4096 + k + 4);
        *reinterpret_cast<f32x4*>(s) = *reinterpret_cast<const f32x4*>(mf + 3072 + k);
        *reinterpret_cast<f32x4*>(s + 4) = *reinterpret_cast<const f32x4*>(mf + 3072 + k + 4);
        *reinterpret_cast<f32x4*>(gb) = *reinterpret_cast<const f32x4*>(mf + 5120 + k);
        *reinterpret_cast<f32x4*>(gb + 4) = *reinterpret_cast<const f32x4*>(mf + 5120 + k + 4);
        u32x4 ov;
#pragma unroll
        for (int j = 0; j < 4; ++j) {
          const float xa = lo2f(xv[j]), xb = hi2f(xv[j]);
          const float ha = rbf((xa - m) * rstd * rbf(1.f + a[2 * j]) + s[2 * j]);
          const float hb = rbf((xb - m) * rstd * rbf(1.f + a[2 * j + 1]) + s[2 * j + 1]);
          ov[j] = pk2(xa + rbf(gb[2 * j] * ha), xb + rbf(gb[2 * j + 1] * hb));
        }
        *reinterpret_cast<u32x4*>(out + (size_t)row * D_ + k) = ov;
      }
    }
  }
}

__global__ __launch_bounds__(256) void k_down(const u16* __restrict__ wd, char* __restrict__ ws, u16* __restrict__ out) {
  __shared__ float gs[2][224];
  __shared__ float red[4][2][64];
  __shared__ int last;
  const int tid = threadIdx.x, lane = tid & 63, wid = tid >> 6;
  const int cg = blockIdx.x & 15, ks = blockIdx.x >> 4;
  const int c8 = tid & 7, kr = tid >> 3;
  u32x4 w[7];
#pragma unroll
  for (int i = 0; i < 7; ++i)
    w[i] = *reinterpret_cast<const u32x4*>(wd + (size_t)(ks * 224 + i * 32 + kr) * D_ + cg * 64 + c8 * 8);
  const float* gup = reinterpret_cast<const float*>(ws + WS_GUP);
  if (tid < 224) {
    const int f = ks * 224 + tid;
#pragma unroll
    for (int r = 0; r < 2; ++r) {
      float g = 0.f, u = 0.f;
#pragma unroll
      for (int p = 0; p < 4; ++p) { g += gup[(size_t)(p * 4 + r) * F_ + f]; u += gup[(size_t)(p * 4 + 2 + r) * F_ + f]; }
      gs[r][tid] = rbf(rbf(silu_f(rbf(g))) * rbf(u));
    }
  }
  __syncthreads();
  float a0[8], a1[8];
#pragma unroll
  for (int j = 0; j < 8; ++j) { a0[j] = 0.f; a1[j] = 0.f; }
#pragma unroll
  for (int i = 0; i < 7; ++i) {
    const float g0 = gs[0][i * 32 + kr], g1 = gs[1][i * 32 + kr];
#pragma unroll
    for (int j = 0; j < 4; ++j) {
      a0[2 * j] += g0 * lo2f(w[i][j]); a0[2 * j + 1] += g0 * hi2f(w[i][j]);
      a1[2 * j] += g1 * lo2f(w[i][j]); a1[2 * j + 1] += g1 * hi2f(w[i][j]);
    }
  }
#pragma unroll
  for (int j = 0; j < 8; ++j) {
    float a = a0[j], b = a1[j];
    a += __shfl_xor(a, 8); a += __shfl_xor(a, 16); a += __shfl_xor(a, 32);
    b += __shfl_xor(b, 8); b += __shfl_xor(b, 16); b += __shfl_xor(b, 32);
    if (lane < 8) { red[wid][0][lane * 8 + j] = a; red[wid][1][lane * 8 + j] = b; }
  }
  __syncthreads();
  float* yp = reinterpret_cast<float*>(ws + WS_YP);
  if (tid < 128) {
    const int r = tid >> 6, cc = tid & 63;
    atomicAdd(yp + (size_t)r * D_ + cg * 64 + cc, red[0][r][cc] + red[1][r][cc] + red[2][r][cc] + red[3][r][cc]);
  }
  asm volatile("s_waitcnt vmcnt(0)" ::: "memory");
  __syncthreads();
  if (tid == 0) {
    const unsigned t = atomicAdd(reinterpret_cast<unsigned*>(ws + WS_CNT) + cg, 1u);
    last = (t == 15u);
  }
  __syncthreads();
  if (last) {
    if (tid < 128) {
      const int r = tid >> 6, cc = tid & 63, n = cg * 64 + cc;
      float y = __hip_atomic_load(yp + (size_t)r * D_ + n, __ATOMIC_RELAXED, __HIP_MEMORY_SCOPE_AGENT);
      const int* sel = reinterpret_cast<const int*>(ws + WS_SEL);
      const int row = sel[r];
      const float rv = reinterpret_cast<const float*>(sel)[2 + r];
      const float sg = rbf(1.f / (1.f + __expf(-rv)));
      y = rbf(rbf(y) * sg);
      const float* rs = reinterpret_cast<const float*>(ws + WS_RS2);
      const float* mf = reinterpret_cast<const float*>(ws + WS_MODF);
      const u16* x1 = reinterpret_cast<const u16*>(ws + WS_X1);
      float m, rstd; row_stats2(rs, row, m, rstd);
      const float xv = bf2f(x1[(size_t)row * D_ + n]);
      const float h2 = rbf((xv - m) * rstd * rbf(1.f + mf[4096 + n]) + mf[3072 + n]);
      const float B = rbf(h2 + y);
      out[(size_t)row * D_ + n] = (u16)(pk2(xv + rbf(mf[5120 + n] * B), 0.f) & 0xffff);
    }
  }
}

extern "C" void kernel_launch(void* const* d_in, const int* in_sizes, int n_in, void* d_out, int out_size,
                              void* d_ws, size_t ws_size, hipStream_t stream) {
  const u16* x = (const u16*)d_in[0];
  const u16* wqkv = (const u16*)d_in[1];
  const u16* wphi = (const u16*)d_in[2];
  const u16* wo = (const u16*)d_in[3];
  const u16* wr = (const u16*)d_in[5];
  const u16* wg = (const u16*)d_in[6];
  const u16* wu = (const u16*)d_in[7];
  const u16* wd = (const u16*)d_in[8];
  const u16* c = (const u16*)d_in[9];
  const u16* wmod = (const u16*)d_in[10];
  char* ws = (char*)d_ws;
  u16* out = (u16*)d_out;
  hipFuncSetAttribute((const void*)k_qkv, hipFuncAttributeMaxDynamicSharedMemorySize, K1_LDS);
  k_prep<<<1920, 256, 0, stream>>>(x, c, wmod, wqkv, wo, ws);
  k_qkv<<<256, 512, K1_LDS, stream>>>(x, wqkv, wphi, ws);
  k_o<<<257, 256, 0, stream>>>(wphi, wr, ws);
  k_wo<<<256, 512, 0, stream>>>(x, wo, ws);
  k_gateup<<<480, 256, 0, stream>>>(wg, wu, ws, out);
  k_down<<<256, 256, 0, stream>>>(wd, ws, out);
}
```

```cpp
#include <hip/hip_runtime.h>
#include <stdint.h>

typedef unsigned short u16;
typedef short bf16x8 __attribute__((ext_vector_type(8)));
typedef short s16x4 __attribute__((ext_vector_type(4)));
typedef float f32x2 __attribute__((ext_vector_type(2)));
typedef float f32x4 __attribute__((ext_vector_type(4)));
typedef float f32x16 __attribute__((ext_vector_type(16)));
typedef unsigned u32x4 __attribute__((ext_vector_type(4)));
typedef unsigned u32x2 __attribute__((ext_vector_type(2)));
typedef __bf16 bf16x2v __attribute__((ext_vector_type(2)));
typedef __attribute__((address_space(3))) s16x4 lds_s16x4;

#define S_ 2048
#define D_ 1024
#define H_ 32
#define F_ 3584

#define WS_MODP   0u
#define WS_STATS  (WS_MODP + 4u*6144u*4u)
#define WS_RS2    (WS_STATS + 2048u*8u)
#define WS_CNT    (WS_RS2 + 3u*2048u*4u)
#define WS_MODF   (WS_CNT + 256u)
#define WS_WR1    (WS_MODF + 6144u*4u)
#define WS_W12    (WS_WR1 + 1024u*4u)
#define WS_SEL    (WS_W12 + 64u)
#define WS_GUP    (WS_SEL + 64u)
#define WS_YP     (WS_GUP + 4u*2u*2u*3584u*4u)
#define WS_Q      (WS_YP + 2u*1024u*4u)
#define WS_O      (WS_Q + 2048u*1024u*2u)
#define WS_X1     (WS_O + 2048u*1024u*2u)
#define WS_KVP    (WS_X1 + 2048u*1024u*2u)
#define WS_WQT    (WS_KVP + 16u*32u*2112u*4u)
#define WS_WOT    (WS_WQT + 1024u*3072u*2u)
#define WS_XN     (WS_WOT + 1024u*1024u*2u)
#define WS_END    (WS_XN + 2048u*1024u*2u)

__device__ __forceinline__ float bf2f(u16 u) { return __uint_as_float(((unsigned)u) << 16); }
__device__ __forceinline__ unsigned pk2(float a, float b) {
  f32x2 f = {a, b}; bf16x2v h = __builtin_convertvector(f, bf16x2v); return __builtin_bit_cast(unsigned, h);
}
__device__ __forceinline__ float rbf(float a) {
  return __uint_as_float(pk2(a, 0.f) << 16);
}
__device__ __forceinline__ float lo2f(unsigned w) { return __uint_as_float(w << 16); }
__device__ __forceinline__ float hi2f(unsigned w) { return __uint_as_float(w & 0xffff0000u); }
__device__ __forceinline__ float wave_sum(float v) {
  for (int o = 32; o > 0; o >>= 1) v += __shfl_xor(v, o);
  return v;
}
__device__ __forceinline__ float silu_f(float x) { return x / (1.f + __expf(-x)); }

__global__ __launch_bounds__(256) void k_prep(const u16* __restrict__ x, const u16* __restrict__ c,
                                              const u16* __restrict__ wmod, const u16* __restrict__ wqkv,
                                              const u16* __restrict__ wo, char* __restrict__ ws) {
  const int tid = threadIdx.x, lane = tid & 63, wid = tid >> 6;
  if (blockIdx.x >= 896) {
    __shared__ __attribute__((aligned(16))) u16 tl[64 * 72];
    const int j = blockIdx.x - 896;
    const u16* W; int N, kb, nb; u16* dst;
    if (j < 768) { W = wqkv; N = 3072; kb = j & 15; nb = j >> 4; dst = reinterpret_cast<u16*>(ws + WS_WQT); }
    else { W = wo; N = 1024; kb = (j - 768) & 15; nb = (j - 768) >> 4; dst = reinterpret_cast<u16*>(ws + WS_WOT); }
    const int r = tid >> 2, cpart = (tid & 3) * 16;
    const u32x4 v0 = *reinterpret_cast<const u32x4*>(W + (size_t)(kb * 64 + r) * N + nb * 64 + cpart);
    const u32x4 v1 = *reinterpret_cast<const u32x4*>(W + (size_t)(kb * 64 + r) * N + nb * 64 + cpart + 8);
    *reinterpret_cast<u32x4*>(tl + r * 72 + cpart) = v0;
    *reinterpret_cast<u32x4*>(tl + r * 72 + cpart + 8) = v1;
    __syncthreads();
    const int lg = lane >> 4, lq = (lane & 15) >> 2, lp = lane & 3;
#pragma unroll
    for (int f = 0; f < 2; ++f) {
      const int fr = wid * 2 + f;
      const int ks = fr >> 2, nt = fr & 3;
      const u16* p = tl + (ks * 32 + lg * 8 + lq) * 72 + nt * 16 + lp * 4;
      const s16x4 lo = __builtin_amdgcn_ds_read_tr16_b64_v4i16((lds_s16x4*)p);
      const s16x4 hi = __builtin_amdgcn_ds_read_tr16_b64_v4i16((lds_s16x4*)(p + 4 * 72));
      const bf16x8 v = __builtin_shufflevector(lo, hi, 0, 1, 2, 3, 4, 5, 6, 7);
      *reinterpret_cast<bf16x8*>(dst + ((size_t)((nb * 4 + nt) * 32 + kb * 2 + ks) * 64 + lane) * 8) = v;
    }
    return;
  }
  if (blockIdx.x < 384) {
    __shared__ float red[4][64];
    const int cg = blockIdx.x % 96, ks = blockIdx.x / 96;
    const int c8 = tid & 7, kr = tid >> 3;
    u32x4 w[8];
    float sc[8];
#pragma unroll
    for (int i = 0; i < 8; ++i) {
      const int k = ks * 256 + i * 32 + kr;
      w[i] = *reinterpret_cast<const u32x4*>(wmod + (size_t)k * 6144 + cg * 64 + c8 * 8);
      sc[i] = rbf(silu_f(bf2f(c[k])));
    }
    float acc[8] = {0, 0, 0, 0, 0, 0, 0, 0};
#pragma unroll
    for (int i = 0; i < 8; ++i)
#pragma unroll
      for (int j = 0; j < 4; ++j) { acc[2 * j] += sc[i] * lo2f(w[i][j]); acc[2 * j + 1] += sc[i] * hi2f(w[i][j]); }
#pragma unroll
    for (int j = 0; j < 8; ++j) {
      float v = acc[j];
      v += __shfl_xor(v, 8); v += __shfl_xor(v, 16); v += __shfl_xor(v, 32);
      acc[j] = v;
    }
    if (lane < 8)
#pragma unroll
      for (int j = 0; j < 8; ++j) red[wid][lane * 8 + j] = acc[j];
    __syncthreads();
    if (tid < 64) {
      float v = red[0][tid] + red[1][tid] + red[2][tid] + red[3][tid];
      reinterpret_cast<float*>(ws + WS_MODP)[ks * 6144 + cg * 64 + tid] = v;
    }
  } else {
    const int row = (blockIdx.x - 384) * 4 + wid;
    const u32x4 a = *reinterpret_cast<const u32x4*>(x + (size_t)row * D_ + lane * 8);
    const u32x4 b = *reinterpret_cast<const u32x4*>(x + (size_t)row * D_ + 512 + lane * 8);
    float s = 0.f;
#pragma unroll
    for (int j = 0; j < 4; ++j) s += lo2f(a[j]) + hi2f(a[j]) + lo2f(b[j]) + hi2f(b[j]);
    const float mean = wave_sum(s) * (1.f / D_);
    float q = 0.f;
#pragma unroll
    for (int j = 0; j < 4; ++j) {
      float d;
      d = lo2f(a[j]) - mean; q += d * d; d = hi2f(a[j]) - mean; q += d * d;
      d = lo2f(b[j]) - mean; q += d * d; d = hi2f(b[j]) - mean; q += d * d;
    }
    const float var = wave_sum(q) * (1.f / D_);
    {
      const float sd = rbf(sqrtf(rbf(rbf(var) + 1e-5f))), mb = rbf(mean);
      u32x4 oa, ob;
#pragma unroll
      for (int j = 0; j < 4; ++j) {
        oa[j] = pk2(rbf(lo2f(a[j]) - mb) / sd, rbf(hi2f(a[j]) - mb) / sd);
        ob[j] = pk2(rbf(lo2f(b[j]) - mb) / sd, rbf(hi2f(b[j]) - mb) / sd);
      }
      u16* xn = reinterpret_cast<u16*>(ws + WS_XN);
      *reinterpret_cast<u32x4*>(xn + (size_t)row * D_ + lane * 8) = oa;
      *reinterpret_cast<u32x4*>(xn + (size_t)row * D_ + 512 + lane * 8) = ob;
    }
    if (lane == 0) {
      float* rs = reinterpret_cast<float*>(ws + WS_RS2);
      rs[row] = 0.f; rs[2048 + row] = 0.f; rs[4096 + row] = 0.f;
    }
    if (blockIdx.x == 384 && tid < 64) reinterpret_cast<unsigned*>(ws + WS_CNT)[tid] = 0u;
    {
      const int bi = blockIdx.x - 384;
      if (bi < 2) reinterpret_cast<f32x4*>(ws + WS_YP)[bi * 256 + tid] = f32x4{0.f, 0.f, 0.f, 0.f};
    }
  }
}

#define A_OFF(row, ch) ((row) * 128 + ((((ch) ^ ((row) & 7))) << 4))
#define B_OFF(row, ch) ((((row) >> 3) * 6 + ((ch) >> 2)) * 512 + ((row) & 7) * 64 + ((((ch) & 3) ^ (((row) >> 2) & 3)) << 4))
#define K1_STAGE 16384
#define K1_MOD (92160)
#define K1_WPHI (K1_MOD + 8192)
#define K1_LDS (K1_WPHI + 4096)
#define C_STRIDE 400
#define PHI_OFF (128 * C_STRIDE)
#define PHI_STRIDE 144

__global__ __launch_bounds__(512) void k_qkv(const u16* __restrict__ x, const u16* __restrict__ wqkv,
                                             const u16* __restrict__ wphi, char* __restrict__ ws) {
  extern __shared__ __attribute__((aligned(16))) char smem[];
  const int tid = threadIdx.x, lane = tid & 63, wid = tid >> 6;
  const int xcd = blockIdx.x & 7, idx = blockIdx.x >> 3;
  const int mt = (xcd & 1) * 8 + (idx & 7), hp = (xcd >> 1) * 4 + (idx >> 3);
  const int row0 = mt * 128;
  float* lmod = reinterpret_cast<float*>(smem + K1_MOD);
  const int sr = tid >> 3, sch = tid & 7;
  const u16* xa0 = reinterpret_cast<const u16*>(ws + WS_XN) + (size_t)(row0 + sr) * D_ + sch * 8;
  const u16* xa1 = xa0 + 64 * D_;
  const int wm = wid & 1, wn = wid >> 1;
  const int l15 = lane & 15, lg = lane >> 4, lq = (lane & 15) >> 2, lp = lane & 3;
  const u16* wf[3];
#pragma unroll
  for (int ni = 0; ni < 3; ++ni) {
    const int nl = wn * 3 + ni;
    const int nt = (nl >> 2) * 64 + hp * 4 + (nl & 3);
    wf[ni] = reinterpret_cast<const u16*>(ws + WS_WQT) + ((size_t)nt * 32 * 64 + lane) * 8;
  }
  u32x4 rg[4][2];
  bf16x8 fb[3][6];
  auto gload = [&](int t) {
    rg[t & 3][0] = *reinterpret_cast<const u32x4*>(xa0 + t * 64);
    rg[t & 3][1] = *reinterpret_cast<const u32x4*>(xa1 + t * 64);
  };
  auto bload = [&](int t) {
#pragma unroll
    for (int ks = 0; ks < 2; ++ks)
#pragma unroll
      for (int ni = 0; ni < 3; ++ni) fb[t % 3][ks * 3 + ni] = *reinterpret_cast<const bf16x8*>(wf[ni] + (size_t)(t * 2 + ks) * 512);
  };
  auto swrite = [&](int t, int buf) {
    char* base = smem + buf * K1_STAGE;
    const u32x4 ga0 = rg[t & 3][0], ga1 = rg[t & 3][1];
    const f32x4 a0 = *reinterpret_cast<const f32x4*>(lmod + t * 64 + sch * 8);
    const f32x4 a1 = *reinterpret_cast<const f32x4*>(lmod + t * 64 + sch * 8 + 4);
    const f32x4 s0 = *reinterpret_cast<const f32x4*>(lmod + 1024 + t * 64 + sch * 8);
    const f32x4 s1 = *reinterpret_cast<const f32x4*>(lmod + 1024 + t * 64 + sch * 8 + 4);
    const float av[8] = {a0[0], a0[1], a0[2], a0[3], a1[0], a1[1], a1[2], a1[3]};
    const float sv[8] = {s0[0], s0[1], s0[2], s0[3], s1[0], s1[1], s1[2], s1[3]};
    u32x4 h0, h1;
#pragma unroll
    for (int j = 0; j < 4; ++j) {
      const unsigned t0 = pk2(lo2f(ga0[j]) * av[2 * j], hi2f(ga0[j]) * av[2 * j + 1]);
      const unsigned t1 = pk2(lo2f(ga1[j]) * av[2 * j], hi2f(ga1[j]) * av[2 * j + 1]);
      h0[j] = pk2(lo2f(t0) + sv[2 * j], hi2f(t0) + sv[2 * j + 1]);
      h1[j] = pk2(lo2f(t1) + sv[2 * j], hi2f(t1) + sv[2 * j + 1]);
    }
    *reinterpret_cast<u32x4*>(base + A_OFF(sr, sch)) = h0;
    *reinterpret_cast<u32x4*>(base + A_OFF(sr + 64, sch)) = h1;
  };
  f32x4 acc[4][3];
#pragma unroll
  for (int i = 0; i < 4; ++i)
#pragma unroll
    for (int j = 0; j < 3; ++j) acc[i][j] = f32x4{0.f, 0.f, 0.f, 0.f};

  gload(0); gload(1); gload(2);
  bload(0); bload(1);
  {
    if (tid < 256) *reinterpret_cast<u32x4*>(smem + K1_WPHI + tid * 16) = *reinterpret_cast<const u32x4*>(wphi + (size_t)hp * 2048 + tid * 8);
    const float* mp = reinterpret_cast<const float*>(ws + WS_MODP);
#pragma unroll
    for (int i = tid; i < 1024; i += 512) {
      const float sa = mp[i] + mp[6144 + i] + mp[2 * 6144 + i] + mp[3 * 6144 + i];
      const float ca = mp[1024 + i] + mp[6144 + 1024 + i] + mp[2 * 6144 + 1024 + i] + mp[3 * 6144 + 1024 + i];
      lmod[i] = rbf(1.f + rbf(ca));
      lmod[1024 + i] = rbf(sa);
    }
  }
  __syncthreads();
  swrite(0, 0);
  __syncthreads();
#pragma unroll
  for (int t = 0; t < 16; ++t) {
    if (t < 13) gload(t + 3);
    if (t < 14) bload(t + 2);
    const char* base = smem + (t & 1) * K1_STAGE;
#pragma unroll
    for (int ks = 0; ks < 2; ++ks) {
      bf16x8 a[4];
#pragma unroll
      for (int mi = 0; mi < 4; ++mi) a[mi] = *reinterpret_cast<const bf16x8*>(base + A_OFF(wm * 64 + mi * 16 + l15, ks * 4 + lg));
#pragma unroll
      for (int mi = 0; mi < 4; ++mi)
#pragma unroll
        for (int ni = 0; ni < 3; ++ni)
          acc[mi][ni] = __builtin_amdgcn_mfma_f32_16x16x32_bf16(fb[t % 3][ks * 3 + ni], a[mi], acc[mi][ni], 0, 0, 0);
    }
    if (t < 15) swrite(t + 1, (t + 1) & 1);
    __syncthreads();
  }
#pragma unroll
  for (int mi = 0; mi < 4; ++mi)
#pragma unroll
    for (int ni = 0; ni < 3; ++ni) {
      const int m = wm * 64 + mi * 16 + l15, n = wn * 48 + ni * 16 + lg * 4;
      u32x2 v = {pk2(acc[mi][ni][0], acc[mi][ni][1]), pk2(acc[mi][ni][2], acc[mi][ni][3])};
      *reinterpret_cast<u32x2*>(smem + m * C_STRIDE + n * 2) = v;
    }
  __syncthreads();
  {
    u16* q = reinterpret_cast<u16*>(ws + WS_Q);
#pragma unroll
    for (int i = 0; i < 2; ++i) {
      const int r = sr + i * 64;
      const u32x4 v = *reinterpret_cast<const u32x4*>(smem + r * C_STRIDE + sch * 16);
      *reinterpret_cast<u32x4*>(q + (size_t)(row0 + r) * D_ + hp * 64 + sch * 8) = v;
    }
  }
  const int l31 = lane & 31, lh = lane >> 5;
  {
    const int hh = wid & 1, rt = wid >> 1;
    const u16* wp = reinterpret_cast<const u16*>(smem + K1_WPHI) + hh * 1024;
    f32x16 xp;
#pragma unroll
    for (int i = 0; i < 16; ++i) xp[i] = 0.f;
#pragma unroll
    for (int s = 0; s < 2; ++s) {
      bf16x8 a;
#pragma unroll
      for (int j = 0; j < 8; ++j) a[j] = (short)wp[(16 * s + 8 * lh + j) * 32 + l31];
      const bf16x8 b = *reinterpret_cast<const bf16x8*>(smem + (rt * 32 + l31) * C_STRIDE + (64 + hh * 32 + 16 * s + 8 * lh) * 2);
      xp = __builtin_amdgcn_mfma_f32_32x32x16_bf16(a, b, xp, 0, 0, 0);
    }
    float mx = -3.0e38f, mn = 3.0e38f;
#pragma unroll
    for (int i = 0; i < 16; ++i) { xp[i] = rbf(xp[i]); mx = fmaxf(mx, xp[i]); mn = fminf(mn, xp[i]); }
    mx = fmaxf(mx, __shfl_xor(mx, 32)); mn = fminf(mn, __shfl_xor(mn, 32));
    float ep[16], en[16], sp = 0.f, sn = 0.f;
#pragma unroll
    for (int i = 0; i < 16; ++i) { ep[i] = __expf(xp[i] - mx); sp += ep[i]; en[i] = __expf(mn - xp[i]); sn += en[i]; }
    sp += __shfl_xor(sp, 32); sn += __shfl_xor(sn, 32);
    const float ip = 1.f / sp, in_ = 1.f / sn;
    char* ph = smem + PHI_OFF + (hh * 128 + rt * 32 + l31) * PHI_STRIDE;
#pragma unroll
    for (int g = 0; g < 4; ++g) {
      u32x2 v = {pk2(ep[4 * g] * ip, ep[4 * g + 1] * ip), pk2(ep[4 * g + 2] * ip, ep[4 * g + 3] * ip)};
      *reinterpret_cast<u32x2*>(ph + (8 * g + 4 * lh) * 2) = v;
      u32x2 w = {pk2(en[4 * g] * in_, en[4 * g + 1] * in_), pk2(en[4 * g + 2] * in_, en[4 * g + 3] * in_)};
      *reinterpret_cast<u32x2*>(ph + 64 + (8 * g + 4 * lh) * 2) = w;
    }
  }
  __syncthreads();
  if (wid < 4) {
    const int hh = wid & 1, ft = wid >> 1;
    const int blk = (lane >> 4) & 1;
    f32x16 kv, zz;
#pragma unroll
    for (int i = 0; i < 16; ++i) { kv[i] = 0.f; zz[i] = 0.f; }
    bf16x8 ones;
#pragma unroll
    for (int j = 0; j < 8; ++j) ones[j] = (short)0x3f80;
#pragma unroll
    for (int s = 0; s < 8; ++s) {
      const char* pa = smem + PHI_OFF + (hh * 128 + 16 * s + 8 * lh + lq) * PHI_STRIDE + (ft * 32 + 16 * blk + 4 * lp) * 2;
      const s16x4 alo = __builtin_amdgcn_ds_read_tr16_b64_v4i16((lds_s16x4*)pa);
      const s16x4 ahi = __builtin_amdgcn_ds_read_tr16_b64_v4i16((lds_s16x4*)(pa + 4 * PHI_STRIDE));
      const bf16x8 a = __builtin_shufflevector(alo, ahi, 0, 1, 2, 3, 4, 5, 6, 7);
      const char* pb = smem + (16 * s + 8 * lh + lq) * C_STRIDE + (128 + hh * 32 + 16 * blk + 4 * lp) * 2;
      const s16x4 blo = __builtin_amdgcn_ds_read_tr16_b64_v4i16((lds_s16x4*)pb);
      const s16x4 bhi = __builtin_amdgcn_ds_read_tr16_b64_v4i16((lds_s16x4*)(pb + 4 * C_STRIDE));
      const bf16x8 b = __builtin_shufflevector(blo, bhi, 0, 1, 2, 3, 4, 5, 6, 7);
      kv = __builtin_amdgcn_mfma_f32_32x32x16_bf16(a, b, kv, 0, 0, 0);
      zz = __builtin_amdgcn_mfma_f32_32x32x16_bf16(a, ones, zz, 0, 0, 0);
    }
    float* kp = reinterpret_cast<float*>(ws + WS_KVP) + (size_t)((hp * 2 + hh) * 16 + mt) * 2112;
#pragma unroll
    for (int i = 0; i < 16; ++i) {
      const int f = ft * 32 + (i & 3) + 8 * (i >> 2) + 4 * lh;
      kp[f * 32 + l31] = kv[i];
      if (l31 == 0) kp[2048 + f] = zz[i];
    }
  }
}

__global__ __launch_bounds__(256) void k_o(const u16* __restrict__ wphi, const u16* __restrict__ wr, char* __restrict__ ws) {
  __shared__ __attribute__((aligned(16))) u16 kvt[32 * 68 + 64];
  __shared__ float zs[64];
  __shared__ float red[8];
  const int tid = threadIdx.x, lane = tid & 63, wid = tid >> 6;
  if (blockIdx.x == 256) {
    const float* mp = reinterpret_cast<const float*>(ws + WS_MODP);
    float* mf = reinterpret_cast<float*>(ws + WS_MODF);
    float* wr1 = reinterpret_cast<float*>(ws + WS_WR1);
    float s1 = 0.f, s2 = 0.f;
#pragma unroll
    for (int i = tid; i < 1024; i += 256) {
#pragma unroll
      for (int q = 0; q < 6; ++q)
        mf[q * 1024 + i] = rbf(mp[q * 1024 + i] + mp[6144 + q * 1024 + i] + mp[2 * 6144 + q * 1024 + i] + mp[3 * 6144 + q * 1024 + i]);
      const float sb = rbf(mp[3072 + i] + mp[6144 + 3072 + i] + mp[2 * 6144 + 3072 + i] + mp[3 * 6144 + 3072 + i]);
      const float cb = rbf(mp[4096 + i] + mp[6144 + 4096 + i] + mp[2 * 6144 + 4096 + i] + mp[3 * 6144 + 4096 + i]);
      const float w = bf2f(wr[i]);
      const float a = rbf(1.f + cb) * w;
      wr1[i] = a; s1 += a; s2 += sb * w;
    }
    s1 = wave_sum(s1); s2 = wave_sum(s2);
    if (lane == 0) { red[wid] = s1; red[4 + wid] = s2; }
    __syncthreads();
    if (tid == 0) {
      float* w12 = reinterpret_cast<float*>(ws + WS_W12);
      w12[0] = red[0] + red[1] + red[2] + red[3];
      w12[1] = red[4] + red[5] + red[6] + red[7];
    }
    return;
  }
  const int h = blockIdx.x & 31, rc = blockIdx.x >> 5;
  const int l31 = lane & 31, lh = lane >> 5;
  const u16* q = reinterpret_cast<const u16*>(ws + WS_Q);
  bf16x8 qb[2][2];
#pragma unroll
  for (int rt = 0; rt < 2; ++rt)
#pragma unroll
    for (int s = 0; s < 2; ++s)
      qb[rt][s] = *reinterpret_cast<const bf16x8*>(q + (size_t)(rc * 256 + wid * 64 + rt * 32 + l31) * D_ + h * 32 + 16 * s + 8 * lh);
  const u16* wp = wphi + (size_t)h * 1024;
  bf16x8 wa[2];
#pragma unroll
  for (int s = 0; s < 2; ++s)
#pragma unroll
    for (int j = 0; j < 8; ++j) wa[s][j] = (short)wp[(16 * s + 8 * lh + j) * 32 + l31];
  {
    const f32x4* kp = reinterpret_cast<const f32x4*>(ws + WS_KVP) + (size_t)h * 16 * 528;
    f32x4 s0 = {0.f, 0.f, 0.f, 0.f}, s1 = s0, s2 = s0;
#pragma unroll
    for (int m = 0; m < 16; ++m) {
      s0 += kp[m * 528 + tid];
      s1 += kp[m * 528 + 256 + tid];
    }
    if (wid == 0) {
#pragma unroll
      for (int m = 0; m < 16; ++m) s2 += kp[m * 528 + 512 + (lane & 15)];
    }
    {
      const int f = tid >> 3, d = (tid & 7) * 4;
#pragma unroll
      for (int j = 0; j < 4; ++j) {
        kvt[(d + j) * 68 + f] = (u16)(pk2(s0[j], 0.f) & 0xffff);
        kvt[(d + j) * 68 + 32 + f] = (u16)(pk2(s1[j], 0.f) & 0xffff);
      }
    }
    if (tid < 16)
#pragma unroll
      for (int j = 0; j < 4; ++j) zs[tid * 4 + j] = rbf(s2[j]);
  }
  __syncthreads();
  bf16x8 ka[4];
#pragma unroll
  for (int s = 0; s < 4; ++s) {
    const s16x4 lo = *reinterpret_cast<const s16x4*>(kvt + l31 * 68 + (s >> 1) * 32 + 16 * (s & 1) + 4 * lh);
    const s16x4 hi = *reinterpret_cast<const s16x4*>(kvt + l31 * 68 + (s >> 1) * 32 + 16 * (s & 1) + 8 + 4 * lh);
    ka[s] = __builtin_shufflevector(lo, hi, 0, 1, 2, 3, 4, 5, 6, 7);
  }
  float zp[16], zn[16];
#pragma unroll
  for (int i = 0; i < 16; ++i) { const int f = (i & 3) + 8 * (i >> 2) + 4 * lh; zp[i] = zs[f]; zn[i] = zs[32 + f]; }
  u16* o = reinterpret_cast<u16*>(ws + WS_O);
#pragma unroll
  for (int rt = 0; rt < 2; ++rt) {
    const int row = rc * 256 + wid * 64 + rt * 32 + l31;
    f32x16 xp;
#pragma unroll
    for (int i = 0; i < 16; ++i) xp[i] = 0.f;
#pragma unroll
    for (int s = 0; s < 2; ++s) {
      xp = __builtin_amdgcn_mfma_f32_32x32x16_bf16(wa[s], qb[rt][s], xp, 0, 0, 0);
    }
    float mx = -3.0e38f, mn = 3.0e38f;
#pragma unroll
    for (int i = 0; i < 16; ++i) { xp[i] = rbf(xp[i]); mx = fmaxf(mx, xp[i]); mn = fminf(mn, xp[i]); }
    mx = fmaxf(mx, __shfl_xor(mx, 32)); mn = fminf(mn, __shfl_xor(mn, 32));
    float ep[16], en[16], sp = 0.f, sn = 0.f;
#pragma unroll
    for (int i = 0; i < 16; ++i) { ep[i] = __expf(xp[i] - mx); sp += ep[i]; en[i] = __expf(mn - xp[i]); sn += en[i]; }
    sp += __shfl_xor(sp, 32); sn += __shfl_xor(sn, 32);
    const float ip = 1.f / sp, in_ = 1.f / sn;
    float den = 0.f;
    bf16x8 pb[4];
#pragma unroll
    for (int s = 0; s < 2; ++s) {
      u32x4 wpk, wnk;
#pragma unroll
      for (int j = 0; j < 4; ++j) {
        wpk[j] = pk2(ep[8 * s + 2 * j] * ip, ep[8 * s + 2 * j + 1] * ip);
        wnk[j] = pk2(en[8 * s + 2 * j] * in_, en[8 * s + 2 * j + 1] * in_);
        den += lo2f(wpk[j]) * zp[8 * s + 2 * j] + hi2f(wpk[j]) * zp[8 * s + 2 * j + 1];
        den += lo2f(wnk[j]) * zn[8 * s + 2 * j] + hi2f(wnk[j]) * zn[8 * s + 2 * j + 1];
      }
      pb[s] = __builtin_bit_cast(bf16x8, wpk);
      pb[2 + s] = __builtin_bit_cast(bf16x8, wnk);
    }
    den += __shfl_xor(den, 32);
    den = rbf(rbf(den) + 1e-6f);
    f32x16 num;
#pragma unroll
    for (int i = 0; i < 16; ++i) num[i] = 0.f;
#pragma unroll
    for (int s = 0; s < 4; ++s) num = __builtin_amdgcn_mfma_f32_32x32x16_bf16(ka[s], pb[s], num, 0, 0, 0);
#pragma unroll
    for (int g = 0; g < 4; ++g) {
      u32x2 v = {pk2(rbf(num[4 * g]) / den, rbf(num[4 * g + 1]) / den), pk2(rbf(num[4 * g + 2]) / den, rbf(num[4 * g + 3]) / den)};
      *reinterpret_cast<u32x2*>(o + (size_t)row * D_ + h * 32 + 8 * g + 4 * lh) = v;
    }
  }
}

#define B3_OFF(row, ch) ((((row) >> 3) * 2 + ((ch) >> 2)) * 512 + ((row) & 7) * 64 + ((((ch) & 3) ^ (((row) >> 2) & 3)) << 4))
#define K3_STAGE 24576
__global__ __launch_bounds__(512) void k_wo(const u16* __restrict__ x, const u16* __restrict__ wo, char* __restrict__ ws) {
  __shared__ __attribute__((aligned(16))) char smem[2 * K3_STAGE];
  const int tid = threadIdx.x, lane = tid & 63, wid = tid >> 6;
  const int xcd = blockIdx.x & 7, idx = blockIdx.x >> 3;
  const int mt = (xcd & 3) * 4 + (idx & 3), nt = (xcd >> 2) * 8 + (idx >> 2);
  const int row0 = mt * 128, col0 = nt * 64;
  const u16* o = reinterpret_cast<const u16*>(ws + WS_O);
  const int sr = tid >> 3, sch = tid & 7;
  const u16* pa0 = o + (size_t)(row0 + sr) * D_ + sch * 8;
  const u16* pa1 = pa0 + 64 * D_;
  const u16* pb = wo + (size_t)sr * D_ + col0 + sch * 8;
  u32x4 rg[4][3];
  auto gload = [&](int t) {
    rg[t & 3][0] = *reinterpret_cast<const u32x4*>(pa0 + t * 64);
    rg[t & 3][1] = *reinterpret_cast<const u32x4*>(pa1 + t * 64);
    rg[t & 3][2] = *reinterpret_cast<const u32x4*>(pb + (size_t)t * 64 * D_);
  };
  auto swrite = [&](int t, int buf) {
    char* base = smem + buf * K3_STAGE;
    const u32x4 ga0 = rg[t & 3][0], ga1 = rg[t & 3][1], gb0 = rg[t & 3][2];
    *reinterpret_cast<u32x4*>(base + A_OFF(sr, sch)) = ga0;
    *reinterpret_cast<u32x4*>(base + A_OFF(sr + 64, sch)) = ga1;
    *reinterpret_cast<u32x4*>(base + 16384 + B3_OFF(sr, sch)) = gb0;
  };
  const int wm = wid & 3, wn = wid >> 2;
  const int l15 = lane & 15, lg = lane >> 4, lq = (lane & 15) >> 2, lp = lane & 3;
  f32x4 acc[2][2];
#pragma unroll
  for (int i = 0; i < 2; ++i)
#pragma unroll
    for (int j = 0; j < 2; ++j) acc[i][j] = f32x4{0.f, 0.f, 0.f, 0.f};
  gload(0); gload(1); gload(2);
  swrite(0, 0);
  __syncthreads();
#pragma unroll
  for (int t = 0; t < 16; ++t) {
    if (t < 13) gload(t + 3);
    const char* base = smem + (t & 1) * K3_STAGE;
#pragma unroll
    for (int ks = 0; ks < 2; ++ks) {
      bf16x8 a[2], b[2];
#pragma unroll
      for (int mi = 0; mi < 2; ++mi) a[mi] = *reinterpret_cast<const bf16x8*>(base + A_OFF(wm * 32 + mi * 16 + l15, ks * 4 + lg));
#pragma unroll
      for (int ni = 0; ni < 2; ++ni) {
        const int ch = wn * 4 + ni * 2 + (lp >> 1);
        const int kr = ks * 32 + lg * 8 + lq;
        const s16x4 lo = __builtin_amdgcn_ds_read_tr16_b64_v4i16((lds_s16x4*)(base + 16384 + B3_OFF(kr, ch) + 8 * (lp & 1)));
        const s16x4 hi = __builtin_amdgcn_ds_read_tr16_b64_v4i16((lds_s16x4*)(base + 16384 + B3_OFF(kr + 4, ch) + 8 * (lp & 1)));
        b[ni] = __builtin_shufflevector(lo, hi, 0, 1, 2, 3, 4, 5, 6, 7);
      }
#pragma unroll
      for (int mi = 0; mi < 2; ++mi)
#pragma unroll
        for (int ni = 0; ni < 2; ++ni)
          acc[mi][ni] = __builtin_amdgcn_mfma_f32_16x16x32_bf16(b[ni], a[mi], acc[mi][ni], 0, 0, 0);
    }
    if (t < 15) swrite(t + 1, (t + 1) & 1);
    __syncthreads();
  }
  const float* mf = reinterpret_cast<const float*>(ws + WS_MODF);
  const float* wr1 = reinterpret_cast<const float*>(ws + WS_WR1);
  u16* x1 = reinterpret_cast<u16*>(ws + WS_X1);
  float* rs = reinterpret_cast<float*>(ws + WS_RS2);
#pragma unroll
  for (int mi = 0; mi < 2; ++mi) {
    const int m = row0 + wm * 32 + mi * 16 + l15;
    float s1 = 0.f, s2 = 0.f, s3 = 0.f;
#pragma unroll
    for (int ni = 0; ni < 2; ++ni) {
      const int n = col0 + wn * 32 + ni * 16 + lg * 4;
      const u32x2 xv = *reinterpret_cast<const u32x2*>(x + (size_t)m * D_ + n);
      const f32x4 g = *reinterpret_cast<const f32x4*>(mf + 2048 + n);
      const f32x4 w = *reinterpret_cast<const f32x4*>(wr1 + n);
      float v[4];
      v[0] = rbf(lo2f(xv[0]) + rbf(g[0] * rbf(acc[mi][ni][0])));
      v[1] = rbf(hi2f(xv[0]) + rbf(g[1] * rbf(acc[mi][ni][1])));
      v[2] = rbf(lo2f(xv[1]) + rbf(g[2] * rbf(acc[mi][ni][2])));
      v[3] = rbf(hi2f(xv[1]) + rbf(g[3] * rbf(acc[mi][ni][3])));
      u32x2 ov = {pk2(v[0], v[1]), pk2(v[2], v[3])};
      *reinterpret_cast<u32x2*>(x1 + (size_t)m * D_ + n) = ov;
#pragma unroll
      for (int j = 0; j < 4; ++j) { s1 += v[j]; s2 += v[j] * v[j]; s3 += v[j] * w[j]; }
    }
    s1 += __shfl_xor(s1, 16); s1 += __shfl_xor(s1, 32);
    s2 += __shfl_xor(s2, 16); s2 += __shfl_xor(s2, 32);
    s3 += __shfl_xor(s3, 16); s3 += __shfl_xor(s3, 32);
    if (lg == 0) { atomicAdd(rs + m, s1); atomicAdd(rs + 2048 + m, s2); atomicAdd(rs + 4096 + m, s3); }
  }
}

__device__ __forceinline__ void row_stats2(const float* rs, int row, float& m, float& rstd) {
  const float mean = rs[row] * (1.f / D_);
  float var = rs[2048 + row] * (1.f / D_) - mean * mean;
  var = fmaxf(var, 0.f);
  m = rbf(mean);
  rstd = rbf(sqrtf(rbf(rbf(var) + 1e-5f)));
}
__device__ __forceinline__ float ln2(float x, float m, float sd, float a, float s) {
  return rbf(rbf(rbf(rbf(x - m) / sd) * a) + s);
}

__global__ __launch_bounds__(256) void k_gateup(const u16* __restrict__ wg, const u16* __restrict__ wu,
                                                char* __restrict__ ws, u16* __restrict__ out) {
  const int tid = threadIdx.x, lane = tid & 63, wid = tid >> 6;
  const float* rs = reinterpret_cast<const float*>(ws + WS_RS2);
  const float* mf = reinterpret_cast<const float*>(ws + WS_MODF);
  const u16* x1 = reinterpret_cast<const u16*>(ws + WS_X1);
  if (blockIdx.x < 224) {
    __shared__ float sv[4][2];
    __shared__ int si[4][2];
    __shared__ float hs[2][256];
    __shared__ float red[4][4][64];
    const int cg = blockIdx.x % 56, ks = blockIdx.x / 56;
    const int c8 = tid & 7, kr = tid >> 3;
    float ra[8], rb[8], rc[8];
#pragma unroll
    for (int i = 0; i < 8; ++i) { ra[i] = rs[i * 256 + tid]; rb[i] = rs[2048 + i * 256 + tid]; rc[i] = rs[4096 + i * 256 + tid]; }
    const float W1 = reinterpret_cast<const float*>(ws + WS_W12)[0], W2 = reinterpret_cast<const float*>(ws + WS_W12)[1];
    const int kk = ks * 256 + tid;
    const float ma = mf[4096 + kk], ms = mf[3072 + kk];
    u32x4 g[8], u[8];
#pragma unroll
    for (int i = 0; i < 8; ++i) {
      const size_t off = (size_t)(ks * 256 + i * 32 + kr) * F_ + cg * 64 + c8 * 8;
      g[i] = *reinterpret_cast<const u32x4*>(wg + off);
      u[i] = *reinterpret_cast<const u32x4*>(wu + off);
    }
    __builtin_amdgcn_sched_barrier(0);
    float v1 = -3.0e38f, v2 = -3.0e38f; int i1 = 0x7fffffff, i2 = 0x7fffffff;
#pragma unroll
    for (int i = 0; i < 8; ++i) {
      const int row = i * 256 + tid;
      const float mean = ra[i] * (1.f / D_);
      const float var = fmaxf(rb[i] * (1.f / D_) - mean * mean, 0.f);
      const float m = rbf(mean), rstd = 1.f / rbf(sqrtf(rbf(rbf(var) + 1e-5f)));
      const float r = rbf(rstd * (rc[i] - m * W1) + W2);
      const bool b1 = r > v1, b2 = r > v2;
      v2 = b1 ? v1 : (b2 ? r : v2); i2 = b1 ? i1 : (b2 ? row : i2);
      v1 = b1 ? r : v1; i1 = b1 ? row : i1;
    }
#pragma unroll
    for (int o = 32; o > 0; o >>= 1) {
      const float a1 = __shfl_xor(v1, o), a2 = __shfl_xor(v2, o);
      const int b1 = __shfl_xor(i1, o), b2 = __shfl_xor(i2, o);
      if (a1 > v1 || (a1 == v1 && b1 < i1)) {
        if (v1 > a2 || (v1 == a2 && i1 < b2)) { v2 = v1; i2 = i1; } else { v2 = a2; i2 = b2; }
        v1 = a1; i1 = b1;
      } else if (a1 > v2 || (a1 == v2 && b1 < i2)) { v2 = a1; i2 = b1; }
    }
    if (lane == 0) { sv[wid][0] = v1; sv[wid][1] = v2; si[wid][0] = i1; si[wid][1] = i2; }
    __syncthreads();
    v1 = sv[0][0]; v2 = sv[0][1]; i1 = si[0][0]; i2 = si[0][1];
#pragma unroll
    for (int w = 1; w < 4; ++w) {
      const float a1 = sv[w][0], a2 = sv[w][1]; const int b1 = si[w][0], b2 = si[w][1];
      if (a1 > v1 || (a1 == v1 && b1 < i1)) {
        if (v1 > a2 || (v1 == a2 && i1 < b2)) { v2 = v1; i2 = i1; } else { v2 = a2; i2 = b2; }
        v1 = a1; i1 = b1;
      } else if (a1 > v2 || (a1 == v2 && b1 < i2)) { v2 = a1; i2 = b1; }
    }
    if (blockIdx.x == 0 && tid == 0) {
      int* sel = reinterpret_cast<int*>(ws + WS_SEL);
      sel[0] = i1; sel[1] = i2;
      reinterpret_cast<float*>(sel)[2] = v1; reinterpret_cast<float*>(sel)[3] = v2;
    }
    {
      const int k = kk;
      const float a = rbf(1.f + ma), s = ms;
      float m, rstd;
      row_stats2(rs, i1, m, rstd);
      hs[0][tid] = ln2(bf2f(x1[(size_t)i1 * D_ + k]), m, rstd, a, s);
      row_stats2(rs, i2, m, rstd);
      hs[1][tid] = ln2(bf2f(x1[(size_t)i2 * D_ + k]), m, rstd, a, s);
    }
    __syncthreads();
    float ag[2][8], au[2][8];
#pragma unroll
    for (int r = 0; r < 2; ++r)
#pragma unroll
      for (int j = 0; j < 8; ++j) { ag[r][j] = 0.f; au[r][j] = 0.f; }
#pragma unroll
    for (int i = 0; i < 8; ++i) {
      const float h0 = hs[0][i * 32 + kr], h1 = hs[1][i * 32 + kr];
#pragma unroll
      for (int j = 0; j < 4; ++j) {
        ag[0][2 * j] += h0 * lo2f(g[i][j]); ag[0][2 * j + 1] += h0 * hi2f(g[i][j]);
        ag[1][2 * j] += h1 * lo2f(g[i][j]); ag[1][2 * j + 1] += h1 * hi2f(g[i][j]);
        au[0][2 * j] += h0 * lo2f(u[i][j]); au[0][2 * j + 1] += h0 * hi2f(u[i][j]);
        au[1][2 * j] += h1 * lo2f(u[i][j]); au[1][2 * j + 1] += h1 * hi2f(u[i][j]);
      }
    }
#pragma unroll
    for (int r = 0; r < 2; ++r)
#pragma unroll
      for (int j = 0; j < 8; ++j) {
        float a = ag[r][j], b = au[r][j];
        a += __shfl_xor(a, 8); a += __shfl_xor(a, 16); a += __shfl_xor(a, 32);
        b += __shfl_xor(b, 8); b += __shfl_xor(b, 16); b += __shfl_xor(b, 32);
        if (lane < 8) { red[wid][r][lane * 8 + j] = a; red[wid][2 + r][lane * 8 + j] = b; }
      }
    __syncthreads();
    {
      const int q = tid >> 6, cc = tid & 63;
      const float v = red[0][q][cc] + red[1][q][cc] + red[2][q][cc] + red[3][q][cc];
      reinterpret_cast<float*>(ws + WS_GUP)[(size_t)(ks * 4 + q) * F_ + cg * 64 + cc] = v;
    }
  } else {
    const int rbase = (blockIdx.x - 224) * 8;
#pragma unroll 1
    for (int rr = wid; rr < 8; rr += 4) {
      const int row = rbase + rr;
      float m, rstd; row_stats2(rs, row, m, rstd);
#pragma unroll
      for (int half = 0; half < 2; ++half) {
        const int k = half * 512 + lane * 8;
        const u32x4 xv = *reinterpret_cast<const u32x4*>(x1 + (size_t)row * D_ + k);
        float a[8], s[8], gb[8];
        *reinterpret_cast<f32x4*>(a) = *reinterpret_cast<const f32x4*>(mf + 4096 + k);
        *reinterpret_cast<f32x4*>(a + 4) = *reinterpret_cast<const f32x4*>(mf + 4096 + k + 4);
        *reinterpret_cast<f32x4*>(s) = *reinterpret_cast<const f32x4*>(mf + 3072 + k);
        *reinterpret_cast<f32x4*>(s + 4) = *reinterpret_cast<const f32x4*>(mf + 3072 + k + 4);
        *reinterpret_cast<f32x4*>(gb) = *reinterpret_cast<const f32x4*>(mf + 5120 + k);
        *reinterpret_cast<f32x4*>(gb + 4) = *reinterpret_cast<const f32x4*>(mf + 5120 + k + 4);
        u32x4 ov;
#pragma unroll
        for (int j = 0; j < 4; ++j) {
          const float xa = lo2f(xv[j]), xb = hi2f(xv[j]);
          const float ha = ln2(xa, m, rstd, rbf(1.f + a[2 * j]), s[2 * j]);
          const float hb = ln2(xb, m, rstd, rbf(1.f + a[2 * j + 1]), s[2 * j + 1]);
          ov[j] = pk2(xa + rbf(gb[2 * j] * ha), xb + rbf(gb[2 * j + 1] * hb));
        }
        *reinterpret_cast<u32x4*>(out + (size_t)row * D_ + k) = ov;
      }
    }
  }
}

__global__ __launch_bounds__(256) void k_down(const u16* __restrict__ wd, char* __restrict__ ws, u16* __restrict__ out) {
  __shared__ float gs[2][224];
  __shared__ float red[4][2][64];
  __shared__ int last;
  const int tid = threadIdx.x, lane = tid & 63, wid = tid >> 6;
  const int cg = blockIdx.x & 15, ks = blockIdx.x >> 4;
  const int c8 = tid & 7, kr = tid >> 3;
  u32x4 w[7];
#pragma unroll
  for (int i = 0; i < 7; ++i)
    w[i] = *reinterpret_cast<const u32x4*>(wd + (size_t)(ks * 224 + i * 32 + kr) * D_ + cg * 64 + c8 * 8);
  const float* gup = reinterpret_cast<const float*>(ws + WS_GUP);
  if (tid < 224) {
    const int f = ks * 224 + tid;
#pragma unroll
    for (int r = 0; r < 2; ++r) {
      float g = 0.f, u = 0.f;
#pragma unroll
      for (int p = 0; p < 4; ++p) { g += gup[(size_t)(p * 4 + r) * F_ + f]; u += gup[(size_t)(p * 4 + 2 + r) * F_ + f]; }
      gs[r][tid] = rbf(rbf(silu_f(rbf(g))) * rbf(u));
    }
  }
  __syncthreads();
  float a0[8], a1[8];
#pragma unroll
  for (int j = 0; j < 8; ++j) { a0[j] = 0.f; a1[j] = 0.f; }
#pragma unroll
  for (int i = 0; i < 7; ++i) {
    const float g0 = gs[0][i * 32 + kr], g1 = gs[1][i * 32 + kr];
#pragma unroll
    for (int j = 0; j < 4; ++j) {
      a0[2 * j] += g0 * lo2f(w[i][j]); a0[2 * j + 1] += g0 * hi2f(w[i][j]);
      a1[2 * j] += g1 * lo2f(w[i][j]); a1[2 * j + 1] += g1 * hi2f(w[i][j]);
    }
  }
#pragma unroll
  for (int j = 0; j < 8; ++j) {
    float a = a0[j], b = a1[j];
    a += __shfl_xor(a, 8); a += __shfl_xor(a, 16); a += __shfl_xor(a, 32);
    b += __shfl_xor(b, 8); b += __shfl_xor(b, 16); b += __shfl_xor(b, 32);
    if (lane < 8) { red[wid][0][lane * 8 + j] = a; red[wid][1][lane * 8 + j] = b; }
  }
  __syncthreads();
  float* yp = reinterpret_cast<float*>(ws + WS_YP);
  if (tid < 128) {
    const int r = tid >> 6, cc = tid & 63;
    atomicAdd(yp + (size_t)r * D_ + cg * 64 + cc, red[0][r][cc] + red[1][r][cc] + red[2][r][cc] + red[3][r][cc]);
  }
  asm volatile("s_waitcnt vmcnt(0)" ::: "memory");
  __syncthreads();
  if (tid == 0) {
    const unsigned t = atomicAdd(reinterpret_cast<unsigned*>(ws + WS_CNT) + cg, 1u);
    last = (t == 15u);
  }
  __syncthreads();
  if (last) {
    if (tid < 128) {
      const int r = tid >> 6, cc = tid & 63, n = cg * 64 + cc;
      float y = __hip_atomic_load(yp + (size_t)r * D_ + n, __ATOMIC_RELAXED, __HIP_MEMORY_SCOPE_AGENT);
      const int* sel = reinterpret_cast<const int*>(ws + WS_SEL);
      const int row = sel[r];
      const float rv = reinterpret_cast<const float*>(sel)[2 + r];
      const float sg = rbf(1.f / (1.f + __expf(-rv)));
      y = rbf(rbf(y) * sg);
      const float* rs = reinterpret_cast<const float*>(ws + WS_RS2);
      const float* mf = reinterpret_cast<const float*>(ws + WS_MODF);
      const u16* x1 = reinterpret_cast<const u16*>(ws + WS_X1);
      float m, rstd; row_stats2(rs, row, m, rstd);
      const float xv = bf2f(x1[(size_t)row * D_ + n]);
      const float h2 = ln2(xv, m, rstd, rbf(1.f + mf[4096 + n]), mf[3072 + n]);
      const float B = rbf(h2 + y);
      out[(size_t)row * D_ + n] = (u16)(pk2(xv + rbf(mf[5120 + n] * B), 0.f) & 0xffff);
    }
  }
}

extern "C" void kernel_launch(void* const* d_in, const int* in_sizes, int n_in, void* d_out, int out_size,
                              void* d_ws, size_t ws_size, hipStream_t stream) {
  const u16* x = (const u16*)d_in[0];
  const u16* wqkv = (const u16*)d_in[1];
  const u16* wphi = (const u16*)d_in[2];
  const u16* wo = (const u16*)d_in[3];
  const u16* wr = (const u16*)d_in[5];
  const u16* wg = (const u16*)d_in[6];
  const u16* wu = (const u16*)d_in[7];
  const u16* wd = (const u16*)d_in[8];
  const u16* c = (const u16*)d_in[9];
  const u16* wmod = (const u16*)d_in[10];
  char* ws = (char*)d_ws;
  u16* out = (u16*)d_out;
  hipFuncSetAttribute((const void*)k_qkv, hipFuncAttributeMaxDynamicSharedMemorySize, K1_LDS);
  k_prep<<<1920, 256, 0, stream>>>(x, c, wmod, wqkv, wo, ws);
  k_qkv<<<256, 512, K1_LDS, stream>>>(x, wqkv, wphi, ws);
  k_o<<<257, 256, 0, stream>>>(wphi, wr, ws);
  k_wo<<<256, 512, 0, stream>>>(x, wo, ws);
  k_gateup<<<480, 256, 0, stream>>>(wg, wu, ws, out);
  k_down<<<256, 256, 0, stream>>>(wd, ws, out);
}
```

```cpp
#include <hip/hip_runtime.h>
#include <stdint.h>

typedef unsigned short u16;
typedef short bf16x8 __attribute__((ext_vector_type(8)));
typedef short s16x4 __attribute__((ext_vector_type(4)));
typedef float f32x2 __attribute__((ext_vector_type(2)));
typedef float f32x4 __attribute__((ext_vector_type(4)));
typedef float f32x16 __attribute__((ext_vector_type(16)));
typedef unsigned u32x4 __attribute__((ext_vector_type(4)));
typedef unsigned u32x2 __attribute__((ext_vector_type(2)));
typedef __bf16 bf16x2v __attribute__((ext_vector_type(2)));
typedef __attribute__((address_space(3))) s16x4 lds_s16x4;

#define S_ 2048
#define D_ 1024
#define H_ 32
#define F_ 3584

#define WS_MODP   0u
#define WS_STATS  (WS_MODP + 4u*6144u*4u)
#define WS_RS2    (WS_STATS + 2048u*8u)
#define WS_CNT    (WS_RS2 + 3u*2048u*4u)
#define WS_MODF   (WS_CNT + 256u)
#define WS_WR1    (WS_MODF + 6144u*4u)
#define WS_W12    (WS_WR1 + 1024u*4u)
#define WS_SEL    (WS_W12 + 64u)
#define WS_GUP    (WS_SEL + 64u)
#define WS_YP     (WS_GUP + 4u*2u*2u*3584u*4u)
#define WS_Q      (WS_YP + 2u*1024u*4u)
#define WS_O      (WS_Q + 2048u*1024u*2u)
#define WS_X1     (WS_O + 2048u*1024u*2u)
#define WS_KVP    (WS_X1 + 2048u*1024u*2u)
#define WS_WQT    (WS_KVP + 16u*32u*2112u*4u)
#define WS_WOT    (WS_WQT + 1024u*3072u*2u)
#define WS_XN     (WS_WOT + 1024u*1024u*2u)
#define WS_HB     (WS_XN + 2048u*1024u*2u)
#define WS_END    (WS_HB + 2048u*1024u*2u)

#define STAMP_AT(i)
__device__ __forceinline__ float bf2f(u16 u) { return __uint_as_float(((unsigned)u) << 16); }
__device__ __forceinline__ unsigned pk2(float a, float b) {
  f32x2 f = {a, b}; bf16x2v h = __builtin_convertvector(f, bf16x2v); return __builtin_bit_cast(unsigned, h);
}
__device__ __forceinline__ float rbf(float a) {
  return __uint_as_float(pk2(a, 0.f) << 16);
}
__device__ __forceinline__ float lo2f(unsigned w) { return __uint_as_float(w << 16); }
__device__ __forceinline__ float hi2f(unsigned w) { return __uint_as_float(w & 0xffff0000u); }
__device__ __forceinline__ float wave_sum(float v) {
  for (int o = 32; o > 0; o >>= 1) v += __shfl_xor(v, o);
  return v;
}
__device__ __forceinline__ float silu_f(float x) { return x / (1.f + __expf(-x)); }

__global__ __launch_bounds__(256) void k_prep(const u16* __restrict__ x, const u16* __restrict__ c,
                                              const u16* __restrict__ wmod, const u16* __restrict__ wqkv,
                                              const u16* __restrict__ wo, char* __restrict__ ws) {
  const int tid = threadIdx.x, lane = tid & 63, wid = tid >> 6;
  if (blockIdx.x < 384) {
    __shared__ float red[4][64];
    const int cg = blockIdx.x % 96, ks = blockIdx.x / 96;
    const int c8 = tid & 7, kr = tid >> 3;
    u32x4 w[8];
    float sc[8];
#pragma unroll
    for (int i = 0; i < 8; ++i) {
      const int k = ks * 256 + i * 32 + kr;
      w[i] = *reinterpret_cast<const u32x4*>(wmod + (size_t)k * 6144 + cg * 64 + c8 * 8);
      sc[i] = rbf(silu_f(bf2f(c[k])));
    }
    float acc[8] = {0, 0, 0, 0, 0, 0, 0, 0};
#pragma unroll
    for (int i = 0; i < 8; ++i)
#pragma unroll
      for (int j = 0; j < 4; ++j) { acc[2 * j] += sc[i] * lo2f(w[i][j]); acc[2 * j + 1] += sc[i] * hi2f(w[i][j]); }
#pragma unroll
    for (int j = 0; j < 8; ++j) {
      float v = acc[j];
      v += __shfl_xor(v, 8); v += __shfl_xor(v, 16); v += __shfl_xor(v, 32);
      acc[j] = v;
    }
    if (lane < 8)
#pragma unroll
      for (int j = 0; j < 8; ++j) red[wid][lane * 8 + j] = acc[j];
    __syncthreads();
    if (tid < 64) {
      float v = red[0][tid] + red[1][tid] + red[2][tid] + red[3][tid];
      reinterpret_cast<float*>(ws + WS_MODP)[ks * 6144 + cg * 64 + tid] = v;
    }
  } else {
    const int row = (blockIdx.x - 384) * 4 + wid;
    const u32x4 a = *reinterpret_cast<const u32x4*>(x + (size_t)row * D_ + lane * 8);
    const u32x4 b = *reinterpret_cast<const u32x4*>(x + (size_t)row * D_ + 512 + lane * 8);
    float s = 0.f;
#pragma unroll
    for (int j = 0; j < 4; ++j) s += lo2f(a[j]) + hi2f(a[j]) + lo2f(b[j]) + hi2f(b[j]);
    const float mean = wave_sum(s) * (1.f / D_);
    float q = 0.f;
#pragma unroll
    for (int j = 0; j < 4; ++j) {
      float d;
      d = lo2f(a[j]) - mean; q += d * d; d = hi2f(a[j]) - mean; q += d * d;
      d = lo2f(b[j]) - mean; q += d * d; d = hi2f(b[j]) - mean; q += d * d;
    }
    const float var = wave_sum(q) * (1.f / D_);
    {
      const float sd = rbf(sqrtf(rbf(rbf(var) + 1e-5f))), mb = rbf(mean);
      u32x4 oa, ob;
#pragma unroll
      for (int j = 0; j < 4; ++j) {
        oa[j] = pk2(rbf(lo2f(a[j]) - mb) / sd, rbf(hi2f(a[j]) - mb) / sd);
        ob[j] = pk2(rbf(lo2f(b[j]) - mb) / sd, rbf(hi2f(b[j]) - mb) / sd);
      }
      u16* xn = reinterpret_cast<u16*>(ws + WS_XN);
      *reinterpret_cast<u32x4*>(xn + (size_t)row * D_ + lane * 8) = oa;
      *reinterpret_cast<u32x4*>(xn + (size_t)row * D_ + 512 + lane * 8) = ob;
    }
    if (lane == 0) {
      float* rs = reinterpret_cast<float*>(ws + WS_RS2);
      rs[row] = 0.f; rs[2048 + row] = 0.f; rs[4096 + row] = 0.f;
    }
    if (blockIdx.x == 384 && tid < 64) reinterpret_cast<unsigned*>(ws + WS_CNT)[tid] = 0u;
    {
      const int bi = blockIdx.x - 384;
      if (bi < 2) reinterpret_cast<f32x4*>(ws + WS_YP)[bi * 256 + tid] = f32x4{0.f, 0.f, 0.f, 0.f};
    }
  }
}

#define A_OFF(row, ch) ((row) * 128 + ((((ch) ^ ((row) & 7))) << 4))
#define B_OFF(row, ch) ((((row) >> 3) * 6 + ((ch) >> 2)) * 512 + ((row) & 7) * 64 + ((((ch) & 3) ^ (((row) >> 2) & 3)) << 4))
#define K1_STAGE 40960
#define K1_MOD (92160)
#define K1_WPHI (K1_MOD + 8192)
#define K1_LDS (K1_WPHI + 4096)
#define C_STRIDE 400
#define PHI_OFF (128 * C_STRIDE)
#define PHI_STRIDE 144

__global__ __launch_bounds__(512) void k_qkv(const u16* __restrict__ x, const u16* __restrict__ wqkv,
                                             const u16* __restrict__ wphi, char* __restrict__ ws) {
  extern __shared__ __attribute__((aligned(16))) char smem[];
  const int tid = threadIdx.x, lane = tid & 63, wid = tid >> 6;
  const int xcd = blockIdx.x & 7, idx = blockIdx.x >> 3;
  const int mt = (xcd & 1) * 8 + (idx & 7), hp = (xcd >> 1) * 4 + (idx >> 3);
  const int row0 = mt * 128;
  STAMP_AT(0);
  float* lmod = reinterpret_cast<float*>(smem + K1_MOD);
  const int lg = lane >> 4, l15 = lane & 15, lq = (lane & 15) >> 2, lp = lane & 3;
  const int sr = tid >> 3, sch = tid & 7;
  const int wm = wid & 1, wn = wid >> 1;
  const u16* xa0 = reinterpret_cast<const u16*>(ws + WS_XN) + (size_t)(row0 + sr) * D_ + sch * 8;
  const u16* xa1 = xa0 + 64 * D_;
  const u16* wb = wqkv + (size_t)sr * 3072 + hp * 64 + sch * 8;
  u32x4 rg[4][5];
  auto gload = [&](int t) {
    u32x4* g = rg[t & 3];
    g[0] = *reinterpret_cast<const u32x4*>(xa0 + t * 64);
    g[1] = *reinterpret_cast<const u32x4*>(xa1 + t * 64);
    const u16* p = wb + (size_t)t * 64 * 3072;
    g[2] = *reinterpret_cast<const u32x4*>(p);
    g[3] = *reinterpret_cast<const u32x4*>(p + 1024);
    g[4] = *reinterpret_cast<const u32x4*>(p + 2048);
  };
  auto swrite = [&](int t, int buf) {
    char* base = smem + buf * K1_STAGE;
    const u32x4 ga0 = rg[t & 3][0], ga1 = rg[t & 3][1];
    const f32x4 a0 = *reinterpret_cast<const f32x4*>(lmod + t * 64 + sch * 8);
    const f32x4 a1 = *reinterpret_cast<const f32x4*>(lmod + t * 64 + sch * 8 + 4);
    const f32x4 s0 = *reinterpret_cast<const f32x4*>(lmod + 1024 + t * 64 + sch * 8);
    const f32x4 s1 = *reinterpret_cast<const f32x4*>(lmod + 1024 + t * 64 + sch * 8 + 4);
    const float av[8] = {a0[0], a0[1], a0[2], a0[3], a1[0], a1[1], a1[2], a1[3]};
    const float sv[8] = {s0[0], s0[1], s0[2], s0[3], s1[0], s1[1], s1[2], s1[3]};
    u32x4 h0, h1;
#pragma unroll
    for (int j = 0; j < 4; ++j) {
      const unsigned t0 = pk2(lo2f(ga0[j]) * av[2 * j], hi2f(ga0[j]) * av[2 * j + 1]);
      const unsigned t1 = pk2(lo2f(ga1[j]) * av[2 * j], hi2f(ga1[j]) * av[2 * j + 1]);
      h0[j] = pk2(lo2f(t0) + sv[2 * j], hi2f(t0) + sv[2 * j + 1]);
      h1[j] = pk2(lo2f(t1) + sv[2 * j], hi2f(t1) + sv[2 * j + 1]);
    }
    *reinterpret_cast<u32x4*>(base + A_OFF(sr, sch)) = h0;
    *reinterpret_cast<u32x4*>(base + A_OFF(sr + 64, sch)) = h1;
    *reinterpret_cast<u32x4*>(base + 16384 + B_OFF(sr, sch)) = rg[t & 3][2];
    *reinterpret_cast<u32x4*>(base + 16384 + B_OFF(sr, 8 + sch)) = rg[t & 3][3];
    *reinterpret_cast<u32x4*>(base + 16384 + B_OFF(sr, 16 + sch)) = rg[t & 3][4];
  };
  f32x4 acc[4][3];
#pragma unroll
  for (int i = 0; i < 4; ++i)
#pragma unroll
    for (int j = 0; j < 3; ++j) acc[i][j] = f32x4{0.f, 0.f, 0.f, 0.f};
  gload(0); gload(1); gload(2);
  {
    if (tid < 256) *reinterpret_cast<u32x4*>(smem + K1_WPHI + tid * 16) = *reinterpret_cast<const u32x4*>(wphi + (size_t)hp * 2048 + tid * 8);
    const float* mp = reinterpret_cast<const float*>(ws + WS_MODP);
#pragma unroll
    for (int i = tid; i < 1024; i += 512) {
      const float sa = mp[i] + mp[6144 + i] + mp[2 * 6144 + i] + mp[3 * 6144 + i];
      const float ca = mp[1024 + i] + mp[6144 + 1024 + i] + mp[2 * 6144 + 1024 + i] + mp[3 * 6144 + 1024 + i];
      lmod[i] = rbf(1.f + rbf(ca));
      lmod[1024 + i] = rbf(sa);
    }
  }
  __syncthreads();
  STAMP_AT(1);
  swrite(0, 0);
  __syncthreads();
  STAMP_AT(2);
#pragma unroll
  for (int t = 0; t < 16; ++t) {
    if (t == 8) STAMP_AT(3);
    if (t < 13) gload(t + 3);
    const char* base = smem + (t & 1) * K1_STAGE;
#pragma unroll
    for (int ks = 0; ks < 2; ++ks) {
      bf16x8 a[4], b[3];
#pragma unroll
      for (int mi = 0; mi < 4; ++mi) a[mi] = *reinterpret_cast<const bf16x8*>(base + A_OFF(wm * 64 + mi * 16 + l15, ks * 4 + lg));
#pragma unroll
      for (int ni = 0; ni < 3; ++ni) {
        const int ch = wn * 6 + ni * 2 + (lp >> 1);
        const int kr = ks * 32 + lg * 8 + lq;
        const s16x4 lo = __builtin_amdgcn_ds_read_tr16_b64_v4i16((lds_s16x4*)(base + 16384 + B_OFF(kr, ch) + 8 * (lp & 1)));
        const s16x4 hi = __builtin_amdgcn_ds_read_tr16_b64_v4i16((lds_s16x4*)(base + 16384 + B_OFF(kr + 4, ch) + 8 * (lp & 1)));
        b[ni] = __builtin_shufflevector(lo, hi, 0, 1, 2, 3, 4, 5, 6, 7);
      }
#pragma unroll
      for (int mi = 0; mi < 4; ++mi)
#pragma unroll
        for (int ni = 0; ni < 3; ++ni)
          acc[mi][ni] = __builtin_amdgcn_mfma_f32_16x16x32_bf16(b[ni], a[mi], acc[mi][ni], 0, 0, 0);
    }
    if (t < 15) swrite(t + 1, (t + 1) & 1);
    __syncthreads();
  }
  STAMP_AT(4);
#pragma unroll
  for (int mi = 0; mi < 4; ++mi)
#pragma unroll
    for (int ni = 0; ni < 3; ++ni) {
      const int m = wm * 64 + mi * 16 + l15, n = wn * 48 + ni * 16 + lg * 4;
      u32x2 v = {pk2(acc[mi][ni][0], acc[mi][ni][1]), pk2(acc[mi][ni][2], acc[mi][ni][3])};
      *reinterpret_cast<u32x2*>(smem + m * C_STRIDE + n * 2) = v;
    }
  __syncthreads();
  {
    u16* q = reinterpret_cast<u16*>(ws + WS_Q);
#pragma unroll
    for (int i = 0; i < 2; ++i) {
      const int r = sr + i * 64;
      const u32x4 v = *reinterpret_cast<const u32x4*>(smem + r * C_STRIDE + sch * 16);
      *reinterpret_cast<u32x4*>(q + (size_t)(row0 + r) * D_ + hp * 64 + sch * 8) = v;
    }
  }
  const int l31 = lane & 31, lh = lane >> 5;
  {
    const int hh = wid & 1, rt = wid >> 1;
    const u16* wp = reinterpret_cast<const u16*>(smem + K1_WPHI) + hh * 1024;
    f32x16 xp;
#pragma unroll
    for (int i = 0; i < 16; ++i) xp[i] = 0.f;
#pragma unroll
    for (int s = 0; s < 2; ++s) {
      bf16x8 a;
#pragma unroll
      for (int j = 0; j < 8; ++j) a[j] = (short)wp[(16 * s + 8 * lh + j) * 32 + l31];
      const bf16x8 b = *reinterpret_cast<const bf16x8*>(smem + (rt * 32 + l31) * C_STRIDE + (64 + hh * 32 + 16 * s + 8 * lh) * 2);
      xp = __builtin_amdgcn_mfma_f32_32x32x16_bf16(a, b, xp, 0, 0, 0);
    }
    float mx = -3.0e38f, mn = 3.0e38f;
#pragma unroll
    for (int i = 0; i < 16; ++i) { xp[i] = rbf(xp[i]); mx = fmaxf(mx, xp[i]); mn = fminf(mn, xp[i]); }
    mx = fmaxf(mx, __shfl_xor(mx, 32)); mn = fminf(mn, __shfl_xor(mn, 32));
    float ep[16], en[16], sp = 0.f, sn = 0.f;
#pragma unroll
    for (int i = 0; i < 16; ++i) { ep[i] = __expf(xp[i] - mx); sp += ep[i]; en[i] = __expf(mn - xp[i]); sn += en[i]; }
    sp += __shfl_xor(sp, 32); sn += __shfl_xor(sn, 32);
    const float ip = 1.f / sp, in_ = 1.f / sn;
    char* ph = smem + PHI_OFF + (hh * 128 + rt * 32 + l31) * PHI_STRIDE;
#pragma unroll
    for (int g = 0; g < 4; ++g) {
      u32x2 v = {pk2(ep[4 * g] * ip, ep[4 * g + 1] * ip), pk2(ep[4 * g + 2] * ip, ep[4 * g + 3] * ip)};
      *reinterpret_cast<u32x2*>(ph + (8 * g + 4 * lh) * 2) = v;
      u32x2 w = {pk2(en[4 * g] * in_, en[4 * g + 1] * in_), pk2(en[4 * g + 2] * in_, en[4 * g + 3] * in_)};
      *reinterpret_cast<u32x2*>(ph + 64 + (8 * g + 4 * lh) * 2) = w;
    }
  }
  __syncthreads();
  if (wid < 4) {
    const int hh = wid & 1, ft = wid >> 1;
    const int blk = (lane >> 4) & 1;
    f32x16 kv, zz;
#pragma unroll
    for (int i = 0; i < 16; ++i) { kv[i] = 0.f; zz[i] = 0.f; }
    bf16x8 ones;
#pragma unroll
    for (int j = 0; j < 8; ++j) ones[j] = (short)0x3f80;
#pragma unroll
    for (int s = 0; s < 8; ++s) {
      const char* pa = smem + PHI_OFF + (hh * 128 + 16 * s + 8 * lh + lq) * PHI_STRIDE + (ft * 32 + 16 * blk + 4 * lp) * 2;
      const s16x4 alo = __builtin_amdgcn_ds_read_tr16_b64_v4i16((lds_s16x4*)pa);
      const s16x4 ahi = __builtin_amdgcn_ds_read_tr16_b64_v4i16((lds_s16x4*)(pa + 4 * PHI_STRIDE));
      const bf16x8 a = __builtin_shufflevector(alo, ahi, 0, 1, 2, 3, 4, 5, 6, 7);
      const char* pb = smem + (16 * s + 8 * lh + lq) * C_STRIDE + (128 + hh * 32 + 16 * blk + 4 * lp) * 2;
      const s16x4 blo = __builtin_amdgcn_ds_read_tr16_b64_v4i16((lds_s16x4*)pb);
      const s16x4 bhi = __builtin_amdgcn_ds_read_tr16_b64_v4i16((lds_s16x4*)(pb + 4 * C_STRIDE));
      const bf16x8 b = __builtin_shufflevector(blo, bhi, 0, 1, 2, 3, 4, 5, 6, 7);
      kv = __builtin_amdgcn_mfma_f32_32x32x16_bf16(a, b, kv, 0, 0, 0);
      zz = __builtin_amdgcn_mfma_f32_32x32x16_bf16(a, ones, zz, 0, 0, 0);
    }
    float* kp = reinterpret_cast<float*>(ws + WS_KVP) + (size_t)((hp * 2 + hh) * 16 + mt) * 2112;
#pragma unroll
    for (int i = 0; i < 16; ++i) {
      const int f = ft * 32 + (i & 3) + 8 * (i >> 2) + 4 * lh;
      kp[f * 32 + l31] = kv[i];
      if (l31 == 0) kp[2048 + f] = zz[i];
    }
  }
  STAMP_AT(5);
}

__global__ __launch_bounds__(256) void k_o(const u16* __restrict__ wphi, const u16* __restrict__ wr, char* __restrict__ ws) {
  __shared__ __attribute__((aligned(16))) u16 kvt[32 * 68 + 64];
  __shared__ float zs[64];
  __shared__ float red[8];
  const int tid = threadIdx.x, lane = tid & 63, wid = tid >> 6;
  if (blockIdx.x == 256) {
    const float* mp = reinterpret_cast<const float*>(ws + WS_MODP);
    float* mf = reinterpret_cast<float*>(ws + WS_MODF);
    float* wr1 = reinterpret_cast<float*>(ws + WS_WR1);
    float s1 = 0.f, s2 = 0.f;
#pragma unroll
    for (int i = tid; i < 1024; i += 256) {
#pragma unroll
      for (int q = 0; q < 6; ++q)
        mf[q * 1024 + i] = rbf(mp[q * 1024 + i] + mp[6144 + q * 1024 + i] + mp[2 * 6144 + q * 1024 + i] + mp[3 * 6144 + q * 1024 + i]);
      const float sb = rbf(mp[3072 + i] + mp[6144 + 3072 + i] + mp[2 * 6144 + 3072 + i] + mp[3 * 6144 + 3072 + i]);
      const float cb = rbf(mp[4096 + i] + mp[6144 + 4096 + i] + mp[2 * 6144 + 4096 + i] + mp[3 * 6144 + 4096 + i]);
      const float w = bf2f(wr[i]);
      const float a = rbf(1.f + cb) * w;
      wr1[i] = a; s1 += a; s2 += sb * w;
    }
    s1 = wave_sum(s1); s2 = wave_sum(s2);
    if (lane == 0) { red[wid] = s1; red[4 + wid] = s2; }
    __syncthreads();
    if (tid == 0) {
      float* w12 = reinterpret_cast<float*>(ws + WS_W12);
      w12[0] = red[0] + red[1] + red[2] + red[3];
      w12[1] = red[4] + red[5] + red[6] + red[7];
    }
    return;
  }
  const int h = blockIdx.x & 31, rc = blockIdx.x >> 5;
  const int l31 = lane & 31, lh = lane >> 5;
  const u16* q = reinterpret_cast<const u16*>(ws + WS_Q);
  bf16x8 qb[2][2];
#pragma unroll
  for (int rt = 0; rt < 2; ++rt)
#pragma unroll
    for (int s = 0; s < 2; ++s)
      qb[rt][s] = *reinterpret_cast<const bf16x8*>(q + (size_t)(rc * 256 + wid * 64 + rt * 32 + l31) * D_ + h * 32 + 16 * s + 8 * lh);
  const u16* wp = wphi + (size_t)h * 1024;
  bf16x8 wa[2];
#pragma unroll
  for (int s = 0; s < 2; ++s)
#pragma unroll
    for (int j = 0; j < 8; ++j) wa[s][j] = (short)wp[(16 * s + 8 * lh + j) * 32 + l31];
  {
    const f32x4* kp = reinterpret_cast<const f32x4*>(ws + WS_KVP) + (size_t)h * 16 * 528;
    f32x4 s0 = {0.f, 0.f, 0.f, 0.f}, s1 = s0, s2 = s0;
#pragma unroll
    for (int m = 0; m < 16; ++m) {
      s0 += kp[m * 528 + tid];
      s1 += kp[m * 528 + 256 + tid];
    }
    if (wid == 0) {
#pragma unroll
      for (int m = 0; m < 16; ++m) s2 += kp[m * 528 + 512 + (lane & 15)];
    }
    {
      const int f = tid >> 3, d = (tid & 7) * 4;
#pragma unroll
      for (int j = 0; j < 4; ++j) {
        kvt[(d + j) * 68 + f] = (u16)(pk2(s0[j], 0.f) & 0xffff);
        kvt[(d + j) * 68 + 32 + f] = (u16)(pk2(s1[j], 0.f) & 0xffff);
      }
    }
    if (tid < 16)
#pragma unroll
      for (int j = 0; j < 4; ++j) zs[tid * 4 + j] = rbf(s2[j]);
  }
  __syncthreads();
  bf16x8 ka[4];
#pragma unroll
  for (int s = 0; s < 4; ++s) {
    const s16x4 lo = *reinterpret_cast<const s16x4*>(kvt + l31 * 68 + (s >> 1) * 32 + 16 * (s & 1) + 4 * lh);
    const s16x4 hi = *reinterpret_cast<const s16x4*>(kvt + l31 * 68 + (s >> 1) * 32 + 16 * (s & 1) + 8 + 4 * lh);
    ka[s] = __builtin_shufflevector(lo, hi, 0, 1, 2, 3, 4, 5, 6, 7);
  }
  float zp[16], zn[16];
#pragma unroll
  for (int i = 0; i < 16; ++i) { const int f = (i & 3) + 8 * (i >> 2) + 4 * lh; zp[i] = zs[f]; zn[i] = zs[32 + f]; }
  u16* o = reinterpret_cast<u16*>(ws + WS_O);
#pragma unroll
  for (int rt = 0; rt < 2; ++rt) {
    const int row = rc * 256 + wid * 64 + rt * 32 + l31;
    f32x16 xp;
#pragma unroll
    for (int i = 0; i < 16; ++i) xp[i] = 0.f;
#pragma unroll
    for (int s = 0; s < 2; ++s) {
      xp = __builtin_amdgcn_mfma_f32_32x32x16_bf16(wa[s], qb[rt][s], xp, 0, 0, 0);
    }
    float mx = -3.0e38f, mn = 3.0e38f;
#pragma unroll
    for (int i = 0; i < 16; ++i) { xp[i] = rbf(xp[i]); mx = fmaxf(mx, xp[i]); mn = fminf(mn, xp[i]); }
    mx = fmaxf(mx, __shfl_xor(mx, 32)); mn = fminf(mn, __shfl_xor(mn, 32));
    float ep[16], en[16], sp = 0.f, sn = 0.f;
#pragma unroll
    for (int i = 0; i < 16; ++i) { ep[i] = __expf(xp[i] - mx); sp += ep[i]; en[i] = __expf(mn - xp[i]); sn += en[i]; }
    sp += __shfl_xor(sp, 32); sn += __shfl_xor(sn, 32);
    const float ip = 1.f / sp, in_ = 1.f / sn;
    float den = 0.f;
    bf16x8 pb[4];
#pragma unroll
    for (int s = 0; s < 2; ++s) {
      u32x4 wpk, wnk;
#pragma unroll
      for (int j = 0; j < 4; ++j) {
        wpk[j] = pk2(ep[8 * s + 2 * j] * ip, ep[8 * s + 2 * j + 1] * ip);
        wnk[j] = pk2(en[8 * s + 2 * j] * in_, en[8 * s + 2 * j + 1] * in_);
        den += lo2f(wpk[j]) * zp[8 * s + 2 * j] + hi2f(wpk[j]) * zp[8 * s + 2 * j + 1];
        den += lo2f(wnk[j]) * zn[8 * s + 2 * j] + hi2f(wnk[j]) * zn[8 * s + 2 * j + 1];
      }
      pb[s] = __builtin_bit_cast(bf16x8, wpk);
      pb[2 + s] = __builtin_bit_cast(bf16x8, wnk);
    }
    den += __shfl_xor(den, 32);
    den = rbf(rbf(den) + 1e-6f);
    f32x16 num;
#pragma unroll
    for (int i = 0; i < 16; ++i) num[i] = 0.f;
#pragma unroll
    for (int s = 0; s < 4; ++s) num = __builtin_amdgcn_mfma_f32_32x32x16_bf16(ka[s], pb[s], num, 0, 0, 0);
#pragma unroll
    for (int g = 0; g < 4; ++g) {
      u32x2 v = {pk2(rbf(num[4 * g]) / den, rbf(num[4 * g + 1]) / den), pk2(rbf(num[4 * g + 2]) / den, rbf(num[4 * g + 3]) / den)};
      *reinterpret_cast<u32x2*>(o + (size_t)row * D_ + h * 32 + 8 * g + 4 * lh) = v;
    }
  }
}

#define B3_OFF(row, ch) ((((row) >> 3) * 2 + ((ch) >> 2)) * 512 + ((row) & 7) * 64 + ((((ch) & 3) ^ (((row) >> 2) & 3)) << 4))
#define K3_STAGE 24576
__global__ __launch_bounds__(512) void k_wo(const u16* __restrict__ x, const u16* __restrict__ wo, char* __restrict__ ws) {
  __shared__ __attribute__((aligned(16))) char smem[4 * K3_STAGE];
  const int tid = threadIdx.x, lane = tid & 63, wid = tid >> 6;
  const int xcd = blockIdx.x & 7, idx = blockIdx.x >> 3;
  const int mt = (xcd & 3) * 4 + (idx & 3), nt = (xcd >> 2) * 8 + (idx >> 2);
  const int row0 = mt * 128, col0 = nt * 64;
  const u16* o = reinterpret_cast<const u16*>(ws + WS_O);
  const int sr = tid >> 3, sch = tid & 7;
  const u16* pa0 = o + (size_t)(row0 + sr) * D_ + sch * 8;
  const u16* pa1 = pa0 + 64 * D_;
  const u16* pb = wo + (size_t)sr * D_ + col0 + sch * 8;
  u32x4 rg[3][6];
  auto gload = [&](int T) {
    u32x4* g = rg[T % 3];
#pragma unroll
    for (int hf = 0; hf < 2; ++hf) {
      const int t = T * 2 + hf;
      g[hf * 3 + 0] = *reinterpret_cast<const u32x4*>(pa0 + t * 64);
      g[hf * 3 + 1] = *reinterpret_cast<const u32x4*>(pa1 + t * 64);
      g[hf * 3 + 2] = *reinterpret_cast<const u32x4*>(pb + (size_t)t * 64 * D_);
    }
  };
  auto swrite = [&](int T) {
    const u32x4* g = rg[T % 3];
#pragma unroll
    for (int hf = 0; hf < 2; ++hf) {
      char* base = smem + ((T & 1) * 2 + hf) * K3_STAGE;
      *reinterpret_cast<u32x4*>(base + A_OFF(sr, sch)) = g[hf * 3 + 0];
      *reinterpret_cast<u32x4*>(base + A_OFF(sr + 64, sch)) = g[hf * 3 + 1];
      *reinterpret_cast<u32x4*>(base + 16384 + B3_OFF(sr, sch)) = g[hf * 3 + 2];
    }
  };
  const int wm = wid & 3, wn = wid >> 2;
  const int l15 = lane & 15, lg = lane >> 4, lq = (lane & 15) >> 2, lp = lane & 3;
  f32x4 acc[2][2];
#pragma unroll
  for (int i = 0; i < 2; ++i)
#pragma unroll
    for (int j = 0; j < 2; ++j) acc[i][j] = f32x4{0.f, 0.f, 0.f, 0.f};
  gload(0); gload(1);
  swrite(0);
  __syncthreads();
#pragma unroll
  for (int T = 0; T < 8; ++T) {
    if (T + 2 < 8) gload(T + 2);
#pragma unroll
    for (int hf = 0; hf < 2; ++hf) {
      const char* base = smem + ((T & 1) * 2 + hf) * K3_STAGE;
#pragma unroll
      for (int ks = 0; ks < 2; ++ks) {
        bf16x8 a[2], b[2];
#pragma unroll
        for (int mi = 0; mi < 2; ++mi) a[mi] = *reinterpret_cast<const bf16x8*>(base + A_OFF(wm * 32 + mi * 16 + l15, ks * 4 + lg));
#pragma unroll
        for (int ni = 0; ni < 2; ++ni) {
          const int ch = wn * 4 + ni * 2 + (lp >> 1);
          const int kr = ks * 32 + lg * 8 + lq;
          const s16x4 lo = __builtin_amdgcn_ds_read_tr16_b64_v4i16((lds_s16x4*)(base + 16384 + B3_OFF(kr, ch) + 8 * (lp & 1)));
          const s16x4 hi = __builtin_amdgcn_ds_read_tr16_b64_v4i16((lds_s16x4*)(base + 16384 + B3_OFF(kr + 4, ch) + 8 * (lp & 1)));
          b[ni] = __builtin_shufflevector(lo, hi, 0, 1, 2, 3, 4, 5, 6, 7);
        }
#pragma unroll
        for (int mi = 0; mi < 2; ++mi)
#pragma unroll
          for (int ni = 0; ni < 2; ++ni)
            acc[mi][ni] = __builtin_amdgcn_mfma_f32_16x16x32_bf16(b[ni], a[mi], acc[mi][ni], 0, 0, 0);
      }
    }
    if (T + 1 < 8) swrite(T + 1);
    __syncthreads();
  }
  const float* mf = reinterpret_cast<const float*>(ws + WS_MODF);
  const float* wr1 = reinterpret_cast<const float*>(ws + WS_WR1);
  u16* x1 = reinterpret_cast<u16*>(ws + WS_X1);
  float* rs = reinterpret_cast<float*>(ws + WS_RS2);
#pragma unroll
  for (int mi = 0; mi < 2; ++mi) {
    const int m = row0 + wm * 32 + mi * 16 + l15;
    float s1 = 0.f, s2 = 0.f, s3 = 0.f;
#pragma unroll
    for (int ni = 0; ni < 2; ++ni) {
      const int n = col0 + wn * 32 + ni * 16 + lg * 4;
      const u32x2 xv = *reinterpret_cast<const u32x2*>(x + (size_t)m * D_ + n);
      const f32x4 g = *reinterpret_cast<const f32x4*>(mf + 2048 + n);
      const f32x4 w = *reinterpret_cast<const f32x4*>(wr1 + n);
      float v[4];
      v[0] = rbf(lo2f(xv[0]) + rbf(g[0] * rbf(acc[mi][ni][0])));
      v[1] = rbf(hi2f(xv[0]) + rbf(g[1] * rbf(acc[mi][ni][1])));
      v[2] = rbf(lo2f(xv[1]) + rbf(g[2] * rbf(acc[mi][ni][2])));
      v[3] = rbf(hi2f(xv[1]) + rbf(g[3] * rbf(acc[mi][ni][3])));
      u32x2 ov = {pk2(v[0], v[1]), pk2(v[2], v[3])};
      *reinterpret_cast<u32x2*>(x1 + (size_t)m * D_ + n) = ov;
#pragma unroll
      for (int j = 0; j < 4; ++j) { s1 += v[j]; s2 += v[j] * v[j]; s3 += v[j] * w[j]; }
    }
    s1 += __shfl_xor(s1, 16); s1 += __shfl_xor(s1, 32);
    s2 += __shfl_xor(s2, 16); s2 += __shfl_xor(s2, 32);
    s3 += __shfl_xor(s3, 16); s3 += __shfl_xor(s3, 32);
    if (lg == 0) { atomicAdd(rs + m, s1); atomicAdd(rs + 2048 + m, s2); atomicAdd(rs + 4096 + m, s3); }
  }
}

__device__ __forceinline__ void row_stats2(const float* rs, int row, float& m, float& rstd) {
  const float mean = rs[row] * (1.f / D_);
  float var = rs[2048 + row] * (1.f / D_) - mean * mean;
  var = fmaxf(var, 0.f);
  m = rbf(mean);
  rstd = rbf(sqrtf(rbf(rbf(var) + 1e-5f)));
}
__device__ __forceinline__ float ln2(float x, float m, float sd, float a, float s) {
  return rbf(rbf(rbf(rbf(x - m) / sd) * a) + s);
}

__global__ __launch_bounds__(256) void k_gateup(const u16* __restrict__ wg, const u16* __restrict__ wu,
                                                char* __restrict__ ws, u16* __restrict__ out) {
  const int tid = threadIdx.x, lane = tid & 63, wid = tid >> 6;
  const float* rs = reinterpret_cast<const float*>(ws + WS_RS2);
  const float* mf = reinterpret_cast<const float*>(ws + WS_MODF);
  const u16* x1 = reinterpret_cast<const u16*>(ws + WS_X1);
  if (blockIdx.x < 224) {
    __shared__ float sv[4][2];
    __shared__ int si[4][2];
    __shared__ float hs[2][256];
    __shared__ float red[4][4][64];
    const int cg = blockIdx.x % 56, ks = blockIdx.x / 56;
    const int c8 = tid & 7, kr = tid >> 3;
    float ra[8], rb[8], rc[8];
#pragma unroll
    for (int i = 0; i < 8; ++i) { ra[i] = rs[i * 256 + tid]; rb[i] = rs[2048 + i * 256 + tid]; rc[i] = rs[4096 + i * 256 + tid]; }
    const float W1 = reinterpret_cast<const float*>(ws + WS_W12)[0], W2 = reinterpret_cast<const float*>(ws + WS_W12)[1];
    const int kk = ks * 256 + tid;
    const float ma = mf[4096 + kk], ms = mf[3072 + kk];
    u32x4 g[8], u[8];
#pragma unroll
    for (int i = 0; i < 8; ++i) {
      const size_t off = (size_t)(ks * 256 + i * 32 + kr) * F_ + cg * 64 + c8 * 8;
      g[i] = *reinterpret_cast<const u32x4*>(wg + off);
      u[i] = *reinterpret_cast<const u32x4*>(wu + off);
    }
    __builtin_amdgcn_sched_barrier(0);
    float v1 = -3.0e38f, v2 = -3.0e38f; int i1 = 0x7fffffff, i2 = 0x7fffffff;
#pragma unroll
    for (int i = 0; i < 8; ++i) {
      const int row = i * 256 + tid;
      const float mean = ra[i] * (1.f / D_);
      const float var = fmaxf(rb[i] * (1.f / D_) - mean * mean, 0.f);
      const float m = rbf(mean), rstd = 1.f / rbf(sqrtf(rbf(rbf(var) + 1e-5f)));
      const float r = rbf(rstd * (rc[i] - m * W1) + W2);
      const bool b1 = r > v1, b2 = r > v2;
      v2 = b1 ? v1 : (b2 ? r : v2); i2 = b1 ? i1 : (b2 ? row : i2);
      v1 = b1 ? r : v1; i1 = b1 ? row : i1;
    }
#pragma unroll
    for (int o = 32; o > 0; o >>= 1) {
      const float a1 = __shfl_xor(v1, o), a2 = __shfl_xor(v2, o);
      const int b1 = __shfl_xor(i1, o), b2 = __shfl_xor(i2, o);
      if (a1 > v1 || (a1 == v1 && b1 < i1)) {
        if (v1 > a2 || (v1 == a2 && i1 < b2)) { v2 = v1; i2 = i1; } else { v2 = a2; i2 = b2; }
        v1 = a1; i1 = b1;
      } else if (a1 > v2 || (a1 == v2 && b1 < i2)) { v2 = a1; i2 = b1; }
    }
    if (lane == 0) { sv[wid][0] = v1; sv[wid][1] = v2; si[wid][0] = i1; si[wid][1] = i2; }
    __syncthreads();
    v1 = sv[0][0]; v2 = sv[0][1]; i1 = si[0][0]; i2 = si[0][1];
#pragma unroll
    for (int w = 1; w < 4; ++w) {
      const float a1 = sv[w][0], a2 = sv[w][1]; const int b1 = si[w][0], b2 = si[w][1];
      if (a1 > v1 || (a1 == v1 && b1 < i1)) {
        if (v1 > a2 || (v1 == a2 && i1 < b2)) { v2 = v1; i2 = i1; } else { v2 = a2; i2 = b2; }
        v1 = a1; i1 = b1;
      } else if (a1 > v2 || (a1 == v2 && b1 < i2)) { v2 = a1; i2 = b1; }
    }
    if (blockIdx.x == 0 && tid == 0) {
      int* sel = reinterpret_cast<int*>(ws + WS_SEL);
      sel[0] = i1; sel[1] = i2;
      reinterpret_cast<float*>(sel)[2] = v1; reinterpret_cast<float*>(sel)[3] = v2;
    }
    {
      const int k = kk;
      const float a = rbf(1.f + ma), s = ms;
      float m, rstd;
      row_stats2(rs, i1, m, rstd);
      hs[0][tid] = ln2(bf2f(x1[(size_t)i1 * D_ + k]), m, rstd, a, s);
      row_stats2(rs, i2, m, rstd);
      hs[1][tid] = ln2(bf2f(x1[(size_t)i2 * D_ + k]), m, rstd, a, s);
    }
    __syncthreads();
    float ag[2][8], au[2][8];
#pragma unroll
    for (int r = 0; r < 2; ++r)
#pragma unroll
      for (int j = 0; j < 8; ++j) { ag[r][j] = 0.f; au[r][j] = 0.f; }
#pragma unroll
    for (int i = 0; i < 8; ++i) {
      const float h0 = hs[0][i * 32 + kr], h1 = hs[1][i * 32 + kr];
#pragma unroll
      for (int j = 0; j < 4; ++j) {
        ag[0][2 * j] += h0 * lo2f(g[i][j]); ag[0][2 * j + 1] += h0 * hi2f(g[i][j]);
        ag[1][2 * j] += h1 * lo2f(g[i][j]); ag[1][2 * j + 1] += h1 * hi2f(g[i][j]);
        au[0][2 * j] += h0 * lo2f(u[i][j]); au[0][2 * j + 1] += h0 * hi2f(u[i][j]);
        au[1][2 * j] += h1 * lo2f(u[i][j]); au[1][2 * j + 1] += h1 * hi2f(u[i][j]);
      }
    }
#pragma unroll
    for (int r = 0; r < 2; ++r)
#pragma unroll
      for (int j = 0; j < 8; ++j) {
        float a = ag[r][j], b = au[r][j];
        a += __shfl_xor(a, 8); a += __shfl_xor(a, 16); a += __shfl_xor(a, 32);
        b += __shfl_xor(b, 8); b += __shfl_xor(b, 16); b += __shfl_xor(b, 32);
        if (lane < 8) { red[wid][r][lane * 8 + j] = a; red[wid][2 + r][lane * 8 + j] = b; }
      }
    __syncthreads();
    {
      const int q = tid >> 6, cc = tid & 63;
      const float v = red[0][q][cc] + red[1][q][cc] + red[2][q][cc] + red[3][q][cc];
      reinterpret_cast<float*>(ws + WS_GUP)[(size_t)(ks * 4 + q) * F_ + cg * 64 + cc] = v;
    }
  } else {
    const int rbase = (blockIdx.x - 224) * 8;
#pragma unroll 1
    for (int rr = wid; rr < 8; rr += 4) {
      const int row = rbase + rr;
      float m, rstd; row_stats2(rs, row, m, rstd);
#pragma unroll
      for (int half = 0; half < 2; ++half) {
        const int k = half * 512 + lane * 8;
        const u32x4 xv = *reinterpret_cast<const u32x4*>(x1 + (size_t)row * D_ + k);
        float a[8], s[8], gb[8];
        *reinterpret_cast<f32x4*>(a) = *reinterpret_cast<const f32x4*>(mf + 4096 + k);
        *reinterpret_cast<f32x4*>(a + 4) = *reinterpret_cast<const f32x4*>(mf + 4096 + k + 4);
        *reinterpret_cast<f32x4*>(s) = *reinterpret_cast<const f32x4*>(mf + 3072 + k);
        *reinterpret_cast<f32x4*>(s + 4) = *reinterpret_cast<const f32x4*>(mf + 3072 + k + 4);
        *reinterpret_cast<f32x4*>(gb) = *reinterpret_cast<const f32x4*>(mf + 5120 + k);
        *reinterpret_cast<f32x4*>(gb + 4) = *reinterpret_cast<const f32x4*>(mf + 5120 + k + 4);
        u32x4 ov;
#pragma unroll
        for (int j = 0; j < 4; ++j) {
          const float xa = lo2f(xv[j]), xb = hi2f(xv[j]);
          const float ha = ln2(xa, m, rstd, rbf(1.f + a[2 * j]), s[2 * j]);
          const float hb = ln2(xb, m, rstd, rbf(1.f + a[2 * j + 1]), s[2 * j + 1]);
          ov[j] = pk2(xa + rbf(gb[2 * j] * ha), xb + rbf(gb[2 * j + 1] * hb));
        }
        *reinterpret_cast<u32x4*>(out + (size_t)row * D_ + k) = ov;
      }
    }
  }
}

__global__ __launch_bounds__(256) void k_down(const u16* __restrict__ wd, char* __restrict__ ws, u16* __restrict__ out) {
  __shared__ float gs[2][224];
  __shared__ float red[4][2][64];
  __shared__ int last;
  const int tid = threadIdx.x, lane = tid & 63, wid = tid >> 6;
  const int cg = blockIdx.x & 15, ks = blockIdx.x >> 4;
  const int c8 = tid & 7, kr = tid >> 3;
  u32x4 w[7];
#pragma unroll
  for (int i = 0; i < 7; ++i)
    w[i] = *reinterpret_cast<const u32x4*>(wd + (size_t)(ks * 224 + i * 32 + kr) * D_ + cg * 64 + c8 * 8);
  const float* gup = reinterpret_cast<const float*>(ws + WS_GUP);
  if (tid < 224) {
    const int f = ks * 224 + tid;
#pragma unroll
    for (int r = 0; r < 2; ++r) {
      float g = 0.f, u = 0.f;
#pragma unroll
      for (int p = 0; p < 4; ++p) { g += gup[(size_t)(p * 4 + r) * F_ + f]; u += gup[(size_t)(p * 4 + 2 + r) * F_ + f]; }
      gs[r][tid] = rbf(rbf(silu_f(rbf(g))) * rbf(u));
    }
  }
  __syncthreads();
  float a0[8], a1[8];
#pragma unroll
  for (int j = 0; j < 8; ++j) { a0[j] = 0.f; a1[j] = 0.f; }
#pragma unroll
  for (int i = 0; i < 7; ++i) {
    const float g0 = gs[0][i * 32 + kr], g1 = gs[1][i * 32 + kr];
#pragma unroll
    for (int j = 0; j < 4; ++j) {
      a0[2 * j] += g0 * lo2f(w[i][j]); a0[2 * j + 1] += g0 * hi2f(w[i][j]);
      a1[2 * j] += g1 * lo2f(w[i][j]); a1[2 * j + 1] += g1 * hi2f(w[i][j]);
    }
  }
#pragma unroll
  for (int j = 0; j < 8; ++j) {
    float a = a0[j], b = a1[j];
    a += __shfl_xor(a, 8); a += __shfl_xor(a, 16); a += __shfl_xor(a, 32);
    b += __shfl_xor(b, 8); b += __shfl_xor(b, 16); b += __shfl_xor(b, 32);
    if (lane < 8) { red[wid][0][lane * 8 + j] = a; red[wid][1][lane * 8 + j] = b; }
  }
  __syncthreads();
  float* yp = reinterpret_cast<float*>(ws + WS_YP);
  if (tid < 128) {
    const int r = tid >> 6, cc = tid & 63;
    atomicAdd(yp + (size_t)r * D_ + cg * 64 + cc, red[0][r][cc] + red[1][r][cc] + red[2][r][cc] + red[3][r][cc]);
  }
  asm volatile("s_waitcnt vmcnt(0)" ::: "memory");
  __syncthreads();
  if (tid == 0) {
    const unsigned t = atomicAdd(reinterpret_cast<unsigned*>(ws + WS_CNT) + cg, 1u);
    last = (t == 15u);
  }
  __syncthreads();
  if (last) {
    if (tid < 128) {
      const int r = tid >> 6, cc = tid & 63, n = cg * 64 + cc;
      float y = __hip_atomic_load(yp + (size_t)r * D_ + n, __ATOMIC_RELAXED, __HIP_MEMORY_SCOPE_AGENT);
      const int* sel = reinterpret_cast<const int*>(ws + WS_SEL);
      const int row = sel[r];
      const float rv = reinterpret_cast<const float*>(sel)[2 + r];
      const float sg = rbf(1.f / (1.f + __expf(-rv)));
      y = rbf(rbf(y) * sg);
      const float* rs = reinterpret_cast<const float*>(ws + WS_RS2);
      const float* mf = reinterpret_cast<const float*>(ws + WS_MODF);
      const u16* x1 = reinterpret_cast<const u16*>(ws + WS_X1);
      float m, rstd; row_stats2(rs, row, m, rstd);
      const float xv = bf2f(x1[(size_t)row * D_ + n]);
      const float h2 = ln2(xv, m, rstd, rbf(1.f + mf[4096 + n]), mf[3072 + n]);
      const float B = rbf(h2 + y);
      out[(size_t)row * D_ + n] = (u16)(pk2(xv + rbf(mf[5120 + n] * B), 0.f) & 0xffff);
    }
  }
}

extern "C" void kernel_launch(void* const* d_in, const int* in_sizes, int n_in, void* d_out, int out_size,
                              void* d_ws, size_t ws_size, hipStream_t stream) {
  const u16* x = (const u16*)d_in[0];
  const u16* wqkv = (const u16*)d_in[1];
  const u16* wphi = (const u16*)d_in[2];
  const u16* wo = (const u16*)d_in[3];
  const u16* wr = (const u16*)d_in[5];
  const u16* wg = (const u16*)d_in[6];
  const u16* wu = (const u16*)d_in[7];
  const u16* wd = (const u16*)d_in[8];
  const u16* c = (const u16*)d_in[9];
  const u16* wmod = (const u16*)d_in[10];
  char* ws = (char*)d_ws;
  u16* out = (u16*)d_out;
  hipFuncSetAttribute((const void*)k_qkv, hipFuncAttributeMaxDynamicSharedMemorySize, K1_LDS);
  k_prep<<<896, 256, 0, stream>>>(x, c, wmod, wqkv, wo, ws);
  k_qkv<<<256, 512, K1_LDS, stream>>>(x, wqkv, wphi, ws);
  k_o<<<257, 256, 0, stream>>>(wphi, wr, ws);
  k_wo<<<256, 512, 0, stream>>>(x, wo, ws);
  k_gateup<<<480, 256, 0, stream>>>(wg, wu, ws, out);
  k_down<<<256, 256, 0, stream>>>(wd, ws, out);
}
```

```cpp
#include <hip/hip_runtime.h>
#include <stdint.h>

typedef unsigned short u16;
typedef short bf16x8 __attribute__((ext_vector_type(8)));
typedef short s16x4 __attribute__((ext_vector_type(4)));
typedef float f32x2 __attribute__((ext_vector_type(2)));
typedef float f32x4 __attribute__((ext_vector_type(4)));
typedef float f32x16 __attribute__((ext_vector_type(16)));
typedef unsigned u32x4 __attribute__((ext_vector_type(4)));
typedef unsigned u32x2 __attribute__((ext_vector_type(2)));
typedef __bf16 bf16x2v __attribute__((ext_vector_type(2)));
typedef __attribute__((address_space(3))) s16x4 lds_s16x4;

#define S_ 2048
#define D_ 1024
#define H_ 32
#define F_ 3584

#define WS_MODP   0u
#define WS_STATS  (WS_MODP + 4u*6144u*4u)
#define WS_RS2    (WS_STATS + 2048u*8u)
#define WS_CNT    (WS_RS2 + 3u*2048u*4u)
#define WS_MODF   (WS_CNT + 256u)
#define WS_WR1    (WS_MODF + 6144u*4u)
#define WS_W12    (WS_WR1 + 1024u*4u)
#define WS_SEL    (WS_W12 + 64u)
#define WS_GUP    (WS_SEL + 64u)
#define WS_YP     (WS_GUP + 4u*2u*2u*3584u*4u)
#define WS_Q      (WS_YP + 2u*1024u*4u)
#define WS_O      (WS_Q + 2048u*1024u*2u)
#define WS_X1     (WS_O + 2048u*1024u*2u)
#define WS_KVP    (WS_X1 + 2048u*1024u*2u)
#define WS_WQT    (WS_KVP + 16u*32u*2112u*4u)
#define WS_WOT    (WS_WQT + 1024u*3072u*2u)
#define WS_XN     (WS_WOT + 1024u*1024u*2u)
#define WS_HB     (WS_XN + 2048u*1024u*2u)
#define WS_END    (WS_HB + 2048u*1024u*2u)

__device__ __forceinline__ float bf2f(u16 u) { return __uint_as_float(((unsigned)u) << 16); }
__device__ __forceinline__ unsigned pk2(float a, float b) {
  f32x2 f = {a, b}; bf16x2v h = __builtin_convertvector(f, bf16x2v); return __builtin_bit_cast(unsigned, h);
}
__device__ __forceinline__ float rbf(float a) {
  return __uint_as_float(pk2(a, 0.f) << 16);
}
__device__ __forceinline__ float lo2f(unsigned w) { return __uint_as_float(w << 16); }
__device__ __forceinline__ float hi2f(unsigned w) { return __uint_as_float(w & 0xffff0000u); }
__device__ __forceinline__ float wave_sum(float v) {
  for (int o = 32; o > 0; o >>= 1) v += __shfl_xor(v, o);
  return v;
}
__device__ __forceinline__ float silu_f(float x) { return x / (1.f + __expf(-x)); }

__global__ __launch_bounds__(256) void k_prep(const u16* __restrict__ x, const u16* __restrict__ c,
                                              const u16* __restrict__ wmod, const u16* __restrict__ wqkv,
                                              const u16* __restrict__ wo, char* __restrict__ ws) {
  const int tid = threadIdx.x, lane = tid & 63, wid = tid >> 6;
  if (blockIdx.x < 384) {
    __shared__ float red[4][64];
    const int cg = blockIdx.x % 96, ks = blockIdx.x / 96;
    const int c8 = tid & 7, kr = tid >> 3;
    u32x4 w[8];
    float sc[8];
#pragma unroll
    for (int i = 0; i < 8; ++i) {
      const int k = ks * 256 + i * 32 + kr;
      w[i] = *reinterpret_cast<const u32x4*>(wmod + (size_t)k * 6144 + cg * 64 + c8 * 8);
      sc[i] = rbf(silu_f(bf2f(c[k])));
    }
    float acc[8] = {0, 0, 0, 0, 0, 0, 0, 0};
#pragma unroll
    for (int i = 0; i < 8; ++i)
#pragma unroll
      for (int j = 0; j < 4; ++j) { acc[2 * j] += sc[i] * lo2f(w[i][j]); acc[2 * j + 1] += sc[i] * hi2f(w[i][j]); }
#pragma unroll
    for (int j = 0; j < 8; ++j) {
      float v = acc[j];
      v += __shfl_xor(v, 8); v += __shfl_xor(v, 16); v += __shfl_xor(v, 32);
      acc[j] = v;
    }
    if (lane < 8)
#pragma unroll
      for (int j = 0; j < 8; ++j) red[wid][lane * 8 + j] = acc[j];
    __syncthreads();
    if (tid < 64) {
      float v = red[0][tid] + red[1][tid] + red[2][tid] + red[3][tid];
      reinterpret_cast<float*>(ws + WS_MODP)[ks * 6144 + cg * 64 + tid] = v;
    }
  } else {
    const int row = (blockIdx.x - 384) * 4 + wid;
    const u32x4 a = *reinterpret_cast<const u32x4*>(x + (size_t)row * D_ + lane * 8);
    const u32x4 b = *reinterpret_cast<const u32x4*>(x + (size_t)row * D_ + 512 + lane * 8);
    float s = 0.f;
#pragma unroll
    for (int j = 0; j < 4; ++j) s += lo2f(a[j]) + hi2f(a[j]) + lo2f(b[j]) + hi2f(b[j]);
    const float mean = wave_sum(s) * (1.f / D_);
    float q = 0.f;
#pragma unroll
    for (int j = 0; j < 4; ++j) {
      float d;
      d = lo2f(a[j]) - mean; q += d * d; d = hi2f(a[j]) - mean; q += d * d;
      d = lo2f(b[j]) - mean; q += d * d; d = hi2f(b[j]) - mean; q += d * d;
    }
    const float var = wave_sum(q) * (1.f / D_);
    {
      const float sd = rbf(sqrtf(rbf(rbf(var) + 1e-5f))), mb = rbf(mean);
      u32x4 oa, ob;
#pragma unroll
      for (int j = 0; j < 4; ++j) {
        oa[j] = pk2(rbf(lo2f(a[j]) - mb) / sd, rbf(hi2f(a[j]) - mb) / sd);
        ob[j] = pk2(rbf(lo2f(b[j]) - mb) / sd, rbf(hi2f(b[j]) - mb) / sd);
      }
      u16* xn = reinterpret_cast<u16*>(ws + WS_XN);
      *reinterpret_cast<u32x4*>(xn + (size_t)row * D_ + lane * 8) = oa;
      *reinterpret_cast<u32x4*>(xn + (size_t)row * D_ + 512 + lane * 8) = ob;
    }
    if (lane == 0) {
      float* rs = reinterpret_cast<float*>(ws + WS_RS2);
      rs[row] = 0.f; rs[2048 + row] = 0.f; rs[4096 + row] = 0.f;
    }
    if (blockIdx.x == 384 && tid < 64) reinterpret_cast<unsigned*>(ws + WS_CNT)[tid] = 0u;
    {
      const int bi = blockIdx.x - 384;
      if (bi < 2) reinterpret_cast<f32x4*>(ws + WS_YP)[bi * 256 + tid] = f32x4{0.f, 0.f, 0.f, 0.f};
    }
  }
}

#define A_OFF(row, ch) ((row) * 128 + ((((ch) ^ ((row) & 7))) << 4))
#define B_OFF(row, ch) ((((row) >> 3) * 6 + ((ch) >> 2)) * 512 + ((row) & 7) * 64 + ((((ch) & 3) ^ (((row) >> 2) & 3)) << 4))
#define K1_STAGE 40960
#define K1_MOD (106496)
#define K1_WPHI (K1_MOD + 8192)
#define K1_LDS (K1_WPHI + 4096)
#define C_STRIDE 400
#define PHI_OFF (128 * C_STRIDE)
#define PHI_STRIDE 144

__global__ __launch_bounds__(512) void k_qkv(const u16* __restrict__ x, const u16* __restrict__ wqkv,
                                             const u16* __restrict__ wphi, char* __restrict__ ws) {
  extern __shared__ __attribute__((aligned(16))) char smem[];
  const int tid = threadIdx.x, lane = tid & 63, wid = tid >> 6;
  const int xcd = blockIdx.x & 7, idx = blockIdx.x >> 3;
  const int mt = (xcd & 1) * 8 + (idx & 7), hp = (xcd >> 1) * 4 + (idx >> 3);
  const int row0 = mt * 128;
  float* lmod = reinterpret_cast<float*>(smem + K1_MOD);
  const int lg = lane >> 4, l15 = lane & 15, lq = (lane & 15) >> 2, lp = lane & 3;
  const int sr = tid >> 3, sch = tid & 7;
  const int wm = wid & 1, wn = wid >> 1;
  const u16* xa0 = reinterpret_cast<const u16*>(ws + WS_XN) + (size_t)(row0 + sr) * D_ + sch * 8;
  const u16* xa1 = xa0 + 64 * D_;
  const u16* bsrc[3];
#pragma unroll
  for (int i = 0; i < 3; ++i) {
    const int st = (wid * 3 + i) * 2 + (lane >> 5);
    const int row = (st / 6) * 8 + ((lane >> 2) & 7);
    const int ch = (st % 6) * 4 + ((lane & 3) ^ ((row >> 2) & 3));
    bsrc[i] = wqkv + (size_t)row * 3072 + (ch >> 3) * 1024 + hp * 64 + (ch & 7) * 8;
  }
  auto bissue = [&](int t) {
    char* base = smem + 32768 + (t % 3) * 24576 + wid * 3072;
#pragma unroll
    for (int i = 0; i < 3; ++i)
      __builtin_amdgcn_global_load_lds((const unsigned*)(bsrc[i] + (size_t)t * 64 * 3072), (unsigned*)(base + i * 1024), 16, 0, 0);
  };
  u32x4 rg[4][2];
  auto gload = [&](int t) {
    rg[t & 3][0] = *reinterpret_cast<const u32x4*>(xa0 + t * 64);
    rg[t & 3][1] = *reinterpret_cast<const u32x4*>(xa1 + t * 64);
  };
  auto swrite = [&](int t, int buf) {
    char* base = smem + buf * 16384;
    const u32x4 ga0 = rg[t & 3][0], ga1 = rg[t & 3][1];
    const f32x4 a0 = *reinterpret_cast<const f32x4*>(lmod + t * 64 + sch * 8);
    const f32x4 a1 = *reinterpret_cast<const f32x4*>(lmod + t * 64 + sch * 8 + 4);
    const f32x4 s0 = *reinterpret_cast<const f32x4*>(lmod + 1024 + t * 64 + sch * 8);
    const f32x4 s1 = *reinterpret_cast<const f32x4*>(lmod + 1024 + t * 64 + sch * 8 + 4);
    const float av[8] = {a0[0], a0[1], a0[2], a0[3], a1[0], a1[1], a1[2], a1[3]};
    const float sv[8] = {s0[0], s0[1], s0[2], s0[3], s1[0], s1[1], s1[2], s1[3]};
    u32x4 h0, h1;
#pragma unroll
    for (int j = 0; j < 4; ++j) {
      const unsigned t0 = pk2(lo2f(ga0[j]) * av[2 * j], hi2f(ga0[j]) * av[2 * j + 1]);
      const unsigned t1 = pk2(lo2f(ga1[j]) * av[2 * j], hi2f(ga1[j]) * av[2 * j + 1]);
      h0[j] = pk2(lo2f(t0) + sv[2 * j], hi2f(t0) + sv[2 * j + 1]);
      h1[j] = pk2(lo2f(t1) + sv[2 * j], hi2f(t1) + sv[2 * j + 1]);
    }
    *reinterpret_cast<u32x4*>(base + A_OFF(sr, sch)) = h0;
    *reinterpret_cast<u32x4*>(base + A_OFF(sr + 64, sch)) = h1;
  };
  f32x4 acc[4][3];
#pragma unroll
  for (int i = 0; i < 4; ++i)
#pragma unroll
    for (int j = 0; j < 3; ++j) acc[i][j] = f32x4{0.f, 0.f, 0.f, 0.f};
  gload(0); gload(1); gload(2);
  bissue(0); bissue(1);
  {
    if (tid < 256) *reinterpret_cast<u32x4*>(smem + K1_WPHI + tid * 16) = *reinterpret_cast<const u32x4*>(wphi + (size_t)hp * 2048 + tid * 8);
    const float* mp = reinterpret_cast<const float*>(ws + WS_MODP);
#pragma unroll
    for (int i = tid; i < 1024; i += 512) {
      const float sa = mp[i] + mp[6144 + i] + mp[2 * 6144 + i] + mp[3 * 6144 + i];
      const float ca = mp[1024 + i] + mp[6144 + 1024 + i] + mp[2 * 6144 + 1024 + i] + mp[3 * 6144 + 1024 + i];
      lmod[i] = rbf(1.f + rbf(ca));
      lmod[1024 + i] = rbf(sa);
    }
  }
  __syncthreads();
  swrite(0, 0);
  __syncthreads();
#pragma unroll
  for (int t = 0; t < 16; ++t) {
    const char* abase = smem + (t & 1) * 16384;
    const char* bbase = smem + 32768 + (t % 3) * 24576;
#pragma unroll
    for (int ks = 0; ks < 2; ++ks) {
      bf16x8 a[4], b[3];
#pragma unroll
      for (int mi = 0; mi < 4; ++mi) a[mi] = *reinterpret_cast<const bf16x8*>(abase + A_OFF(wm * 64 + mi * 16 + l15, ks * 4 + lg));
#pragma unroll
      for (int ni = 0; ni < 3; ++ni) {
        const int ch = wn * 6 + ni * 2 + (lp >> 1);
        const int kr = ks * 32 + lg * 8 + lq;
        const s16x4 lo = __builtin_amdgcn_ds_read_tr16_b64_v4i16((lds_s16x4*)(bbase + B_OFF(kr, ch) + 8 * (lp & 1)));
        const s16x4 hi = __builtin_amdgcn_ds_read_tr16_b64_v4i16((lds_s16x4*)(bbase + B_OFF(kr + 4, ch) + 8 * (lp & 1)));
        b[ni] = __builtin_shufflevector(lo, hi, 0, 1, 2, 3, 4, 5, 6, 7);
      }
#pragma unroll
      for (int mi = 0; mi < 4; ++mi)
#pragma unroll
        for (int ni = 0; ni < 3; ++ni)
          acc[mi][ni] = __builtin_amdgcn_mfma_f32_16x16x32_bf16(b[ni], a[mi], acc[mi][ni], 0, 0, 0);
    }
    if (t < 15) swrite(t + 1, (t + 1) & 1);
    asm volatile("s_waitcnt vmcnt(0)" ::: "memory");
    if (t < 13) gload(t + 3);
    if (t < 14) bissue(t + 2);
    asm volatile("s_waitcnt lgkmcnt(0)" ::: "memory");
    __builtin_amdgcn_s_barrier();
  }
#pragma unroll
  for (int mi = 0; mi < 4; ++mi)
#pragma unroll
    for (int ni = 0; ni < 3; ++ni) {
      const int m = wm * 64 + mi * 16 + l15, n = wn * 48 + ni * 16 + lg * 4;
      u32x2 v = {pk2(acc[mi][ni][0], acc[mi][ni][1]), pk2(acc[mi][ni][2], acc[mi][ni][3])};
      *reinterpret_cast<u32x2*>(smem + m * C_STRIDE + n * 2) = v;
    }
  __syncthreads();
  {
    u16* q = reinterpret_cast<u16*>(ws + WS_Q);
#pragma unroll
    for (int i = 0; i < 2; ++i) {
      const int r = sr + i * 64;
      const u32x4 v = *reinterpret_cast<const u32x4*>(smem + r * C_STRIDE + sch * 16);
      *reinterpret_cast<u32x4*>(q + (size_t)(row0 + r) * D_ + hp * 64 + sch * 8) = v;
    }
  }
  const int l31 = lane & 31, lh = lane >> 5;
  {
    const int hh = wid & 1, rt = wid >> 1;
    const u16* wp = reinterpret_cast<const u16*>(smem + K1_WPHI) + hh * 1024;
    f32x16 xp;
#pragma unroll
    for (int i = 0; i < 16; ++i) xp[i] = 0.f;
#pragma unroll
    for (int s = 0; s < 2; ++s) {
      bf16x8 a;
#pragma unroll
      for (int j = 0; j < 8; ++j) a[j] = (short)wp[(16 * s + 8 * lh + j) * 32 + l31];
      const bf16x8 b = *reinterpret_cast<const bf16x8*>(smem + (rt * 32 + l31) * C_STRIDE + (64 + hh * 32 + 16 * s + 8 * lh) * 2);
      xp = __builtin_amdgcn_mfma_f32_32x32x16_bf16(a, b, xp, 0, 0, 0);
    }
    float mx = -3.0e38f, mn = 3.0e38f;
#pragma unroll
    for (int i = 0; i < 16; ++i) { xp[i] = rbf(xp[i]); mx = fmaxf(mx, xp[i]); mn = fminf(mn, xp[i]); }
    mx = fmaxf(mx, __shfl_xor(mx, 32)); mn = fminf(mn, __shfl_xor(mn, 32));
    float ep[16], en[16], sp = 0.f, sn = 0.f;
#pragma unroll
    for (int i = 0; i < 16; ++i) { ep[i] = __expf(xp[i] - mx); sp += ep[i]; en[i] = __expf(mn - xp[i]); sn += en[i]; }
    sp += __shfl_xor(sp, 32); sn += __shfl_xor(sn, 32);
    const float ip = 1.f / sp, in_ = 1.f / sn;
    char* ph = smem + PHI_OFF + (hh * 128 + rt * 32 + l31) * PHI_STRIDE;
#pragma unroll
    for (int g = 0; g < 4; ++g) {
      u32x2 v = {pk2(ep[4 * g] * ip, ep[4 * g + 1] * ip), pk2(ep[4 * g + 2] * ip, ep[4 * g + 3] * ip)};
      *reinterpret_cast<u32x2*>(ph + (8 * g + 4 * lh) * 2) = v;
      u32x2 w = {pk2(en[4 * g] * in_, en[4 * g + 1] * in_), pk2(en[4 * g + 2] * in_, en[4 * g + 3] * in_)};
      *reinterpret_cast<u32x2*>(ph + 64 + (8 * g + 4 * lh) * 2) = w;
    }
  }
  __syncthreads();
  if (wid < 4) {
    const int hh = wid & 1, ft = wid >> 1;
    const int blk = (lane >> 4) & 1;
    f32x16 kv, zz;
#pragma unroll
    for (int i = 0; i < 16; ++i) { kv[i] = 0.f; zz[i] = 0.f; }
    bf16x8 ones;
#pragma unroll
    for (int j = 0; j < 8; ++j) ones[j] = (short)0x3f80;
#pragma unroll
    for (int s = 0; s < 8; ++s) {
      const char* pa = smem + PHI_OFF + (hh * 128 + 16 * s + 8 * lh + lq) * PHI_STRIDE + (ft * 32 + 16 * blk + 4 * lp) * 2;
      const s16x4 alo = __builtin_amdgcn_ds_read_tr16_b64_v4i16((lds_s16x4*)pa);
      const s16x4 ahi = __builtin_amdgcn_ds_read_tr16_b64_v4i16((lds_s16x4*)(pa + 4 * PHI_STRIDE));
      const bf16x8 a = __builtin_shufflevector(alo, ahi, 0, 1, 2, 3, 4, 5, 6, 7);
      const char* pb = smem + (16 * s + 8 * lh + lq) * C_STRIDE + (128 + hh * 32 + 16 * blk + 4 * lp) * 2;
      const s16x4 blo = __builtin_amdgcn_ds_read_tr16_b64_v4i16((lds_s16x4*)pb);
      const s16x4 bhi = __builtin_amdgcn_ds_read_tr16_b64_v4i16((lds_s16x4*)(pb + 4 * C_STRIDE));
      const bf16x8 b = __builtin_shufflevector(blo, bhi, 0, 1, 2, 3, 4, 5, 6, 7);
      kv = __builtin_amdgcn_mfma_f32_32x32x16_bf16(a, b, kv, 0, 0, 0);
      zz = __builtin_amdgcn_mfma_f32_32x32x16_bf16(a, ones, zz, 0, 0, 0);
    }
    float* kp = reinterpret_cast<float*>(ws + WS_KVP) + (size_t)((hp * 2 + hh) * 16 + mt) * 2112;
#pragma unroll
    for (int i = 0; i < 16; ++i) {
      const int f = ft * 32 + (i & 3) + 8 * (i >> 2) + 4 * lh;
      kp[f * 32 + l31] = kv[i];
      if (l31 == 0) kp[2048 + f] = zz[i];
    }
  }
}

__global__ __launch_bounds__(256) void k_o(const u16* __restrict__ wphi, const u16* __restrict__ wr, char* __restrict__ ws) {
  __shared__ __attribute__((aligned(16))) u16 kvt[32 * 68 + 64];
  __shared__ float zs[64];
  __shared__ float red[8];
  const int tid = threadIdx.x, lane = tid & 63, wid = tid >> 6;
  if (blockIdx.x == 256) {
    const float* mp = reinterpret_cast<const float*>(ws + WS_MODP);
    float* mf = reinterpret_cast<float*>(ws + WS_MODF);
    float* wr1 = reinterpret_cast<float*>(ws + WS_WR1);
    float s1 = 0.f, s2 = 0.f;
#pragma unroll
    for (int i = tid; i < 1024; i += 256) {
#pragma unroll
      for (int q = 0; q < 6; ++q)
        mf[q * 1024 + i] = rbf(mp[q * 1024 + i] + mp[6144 + q * 1024 + i] + mp[2 * 6144 + q * 1024 + i] + mp[3 * 6144 + q * 1024 + i]);
      const float sb = rbf(mp[3072 + i] + mp[6144 + 3072 + i] + mp[2 * 6144 + 3072 + i] + mp[3 * 6144 + 3072 + i]);
      const float cb = rbf(mp[4096 + i] + mp[6144 + 4096 + i] + mp[2 * 6144 + 4096 + i] + mp[3 * 6144 + 4096 + i]);
      const float w = bf2f(wr[i]);
      const float a = rbf(1.f + cb) * w;
      wr1[i] = a; s1 += a; s2 += sb * w;
    }
    s1 = wave_sum(s1); s2 = wave_sum(s2);
    if (lane == 0) { red[wid] = s1; red[4 + wid] = s2; }
    __syncthreads();
    if (tid == 0) {
      float* w12 = reinterpret_cast<float*>(ws + WS_W12);
      w12[0] = red[0] + red[1] + red[2] + red[3];
      w12[1] = red[4] + red[5] + red[6] + red[7];
    }
    return;
  }
  const int h = blockIdx.x & 31, rc = blockIdx.x >> 5;
  const int l31 = lane & 31, lh = lane >> 5;
  const u16* q = reinterpret_cast<const u16*>(ws + WS_Q);
  bf16x8 qb[2][2];
#pragma unroll
  for (int rt = 0; rt < 2; ++rt)
#pragma unroll
    for (int s = 0; s < 2; ++s)
      qb[rt][s] = *reinterpret_cast<const bf16x8*>(q + (size_t)(rc * 256 + wid * 64 + rt * 32 + l31) * D_ + h * 32 + 16 * s + 8 * lh);
  const u16* wp = wphi + (size_t)h * 1024;
  bf16x8 wa[2];
#pragma unroll
  for (int s = 0; s < 2; ++s)
#pragma unroll
    for (int j = 0; j < 8; ++j) wa[s][j] = (short)wp[(16 * s + 8 * lh + j) * 32 + l31];
  {
    const f32x4* kp = reinterpret_cast<const f32x4*>(ws + WS_KVP) + (size_t)h * 16 * 528;
    f32x4 s0 = {0.f, 0.f, 0.f, 0.f}, s1 = s0, s2 = s0;
#pragma unroll
    for (int m = 0; m < 16; ++m) {
      s0 += kp[m * 528 + tid];
      s1 += kp[m * 528 + 256 + tid];
    }
    if (wid == 0) {
#pragma unroll
      for (int m = 0; m < 16; ++m) s2 += kp[m * 528 + 512 + (lane & 15)];
    }
    {
      const int f = tid >> 3, d = (tid & 7) * 4;
#pragma unroll
      for (int j = 0; j < 4; ++j) {
        kvt[(d + j) * 68 + f] = (u16)(pk2(s0[j], 0.f) & 0xffff);
        kvt[(d + j) * 68 + 32 + f] = (u16)(pk2(s1[j], 0.f) & 0xffff);
      }
    }
    if (tid < 16)
#pragma unroll
      for (int j = 0; j < 4; ++j) zs[tid * 4 + j] = rbf(s2[j]);
  }
  __syncthreads();
  bf16x8 ka[4];
#pragma unroll
  for (int s = 0; s < 4; ++s) {
    const s16x4 lo = *reinterpret_cast<const s16x4*>(kvt + l31 * 68 + (s >> 1) * 32 + 16 * (s & 1) + 4 * lh);
    const s16x4 hi = *reinterpret_cast<const s16x4*>(kvt + l31 * 68 + (s >> 1) * 32 + 16 * (s & 1) + 8 + 4 * lh);
    ka[s] = __builtin_shufflevector(lo, hi, 0, 1, 2, 3, 4, 5, 6, 7);
  }
  float zp[16], zn[16];
#pragma unroll
  for (int i = 0; i < 16; ++i) { const int f = (i & 3) + 8 * (i >> 2) + 4 * lh; zp[i] = zs[f]; zn[i] = zs[32 + f]; }
  u16* o = reinterpret_cast<u16*>(ws + WS_O);
#pragma unroll
  for (int rt = 0; rt < 2; ++rt) {
    const int row = rc * 256 + wid * 64 + rt * 32 + l31;
    f32x16 xp;
#pragma unroll
    for (int i = 0; i < 16; ++i) xp[i] = 0.f;
#pragma unroll
    for (int s = 0; s < 2; ++s) {
      xp = __builtin_amdgcn_mfma_f32_32x32x16_bf16(wa[s], qb[rt][s], xp, 0, 0, 0);
    }
    float mx = -3.0e38f, mn = 3.0e38f;
#pragma unroll
    for (int i = 0; i < 16; ++i) { xp[i] = rbf(xp[i]); mx = fmaxf(mx, xp[i]); mn = fminf(mn, xp[i]); }
    mx = fmaxf(mx, __shfl_xor(mx, 32)); mn = fminf(mn, __shfl_xor(mn, 32));
    float ep[16], en[16], sp = 0.f, sn = 0.f;
#pragma unroll
    for (int i = 0; i < 16; ++i) { ep[i] = __expf(xp[i] - mx); sp += ep[i]; en[i] = __expf(mn - xp[i]); sn += en[i]; }
    sp += __shfl_xor(sp, 32); sn += __shfl_xor(sn, 32);
    const float ip = 1.f / sp, in_ = 1.f / sn;
    float den = 0.f;
    bf16x8 pb[4];
#pragma unroll
    for (int s = 0; s < 2; ++s) {
      u32x4 wpk, wnk;
#pragma unroll
      for (int j = 0; j < 4; ++j) {
        wpk[j] = pk2(ep[8 * s + 2 * j] * ip, ep[8 * s + 2 * j + 1] * ip);
        wnk[j] = pk2(en[8 * s + 2 * j] * in_, en[8 * s + 2 * j + 1] * in_);
        den += lo2f(wpk[j]) * zp[8 * s + 2 * j] + hi2f(wpk[j]) * zp[8 * s + 2 * j + 1];
        den += lo2f(wnk[j]) * zn[8 * s + 2 * j] + hi2f(wnk[j]) * zn[8 * s + 2 * j + 1];
      }
      pb[s] = __builtin_bit_cast(bf16x8, wpk);
      pb[2 + s] = __builtin_bit_cast(bf16x8, wnk);
    }
    den += __shfl_xor(den, 32);
    den = rbf(rbf(den) + 1e-6f);
    f32x16 num;
#pragma unroll
    for (int i = 0; i < 16; ++i) num[i] = 0.f;
#pragma unroll
    for (int s = 0; s < 4; ++s) num = __builtin_amdgcn_mfma_f32_32x32x16_bf16(ka[s], pb[s], num, 0, 0, 0);
#pragma unroll
    for (int g = 0; g < 4; ++g) {
      u32x2 v = {pk2(rbf(num[4 * g]) / den, rbf(num[4 * g + 1]) / den), pk2(rbf(num[4 * g + 2]) / den, rbf(num[4 * g + 3]) / den)};
      *reinterpret_cast<u32x2*>(o + (size_t)row * D_ + h * 32 + 8 * g + 4 * lh) = v;
    }
  }
}

#define B3_OFF(row, ch) ((((row) >> 3) * 2 + ((ch) >> 2)) * 512 + ((row) & 7) * 64 + ((((ch) & 3) ^ (((row) >> 2) & 3)) << 4))
#define K3_STAGE 24576
__global__ __launch_bounds__(512) void k_wo(const u16* __restrict__ x, const u16* __restrict__ wo, char* __restrict__ ws) {
  __shared__ __attribute__((aligned(16))) char smem[4 * K3_STAGE];
  const int tid = threadIdx.x, lane = tid & 63, wid = tid >> 6;
  const int xcd = blockIdx.x & 7, idx = blockIdx.x >> 3;
  const int mt = (xcd & 3) * 4 + (idx & 3), nt = (xcd >> 2) * 8 + (idx >> 2);
  const int row0 = mt * 128, col0 = nt * 64;
  const u16* o = reinterpret_cast<const u16*>(ws + WS_O);
  const int sr = tid >> 3, sch = tid & 7;
  const u16* pa0 = o + (size_t)(row0 + sr) * D_ + sch * 8;
  const u16* pa1 = pa0 + 64 * D_;
  const u16* pb = wo + (size_t)sr * D_ + col0 + sch * 8;
  u32x4 rg[3][6];
  auto gload = [&](int T) {
    u32x4* g = rg[T % 3];
#pragma unroll
    for (int hf = 0; hf < 2; ++hf) {
      const int t = T * 2 + hf;
      g[hf * 3 + 0] = *reinterpret_cast<const u32x4*>(pa0 + t * 64);
      g[hf * 3 + 1] = *reinterpret_cast<const u32x4*>(pa1 + t * 64);
      g[hf * 3 + 2] = *reinterpret_cast<const u32x4*>(pb + (size_t)t * 64 * D_);
    }
  };
  auto swrite = [&](int T) {
    const u32x4* g = rg[T % 3];
#pragma unroll
    for (int hf = 0; hf < 2; ++hf) {
      char* base = smem + ((T & 1) * 2 + hf) * K3_STAGE;
      *reinterpret_cast<u32x4*>(base + A_OFF(sr, sch)) = g[hf * 3 + 0];
      *reinterpret_cast<u32x4*>(base + A_OFF(sr + 64, sch)) = g[hf * 3 + 1];
      *reinterpret_cast<u32x4*>(base + 16384 + B3_OFF(sr, sch)) = g[hf * 3 + 2];
    }
  };
  const int wm = wid & 3, wn = wid >> 2;
  const int l15 = lane & 15, lg = lane >> 4, lq = (lane & 15) >> 2, lp = lane & 3;
  f32x4 acc[2][2];
#pragma unroll
  for (int i = 0; i < 2; ++i)
#pragma unroll
    for (int j = 0; j < 2; ++j) acc[i][j] = f32x4{0.f, 0.f, 0.f, 0.f};
  gload(0); gload(1);
  swrite(0);
  __syncthreads();
#pragma unroll
  for (int T = 0; T < 8; ++T) {
    if (T + 2 < 8) gload(T + 2);
#pragma unroll
    for (int hf = 0; hf < 2; ++hf) {
      const char* base = smem + ((T & 1) * 2 + hf) * K3_STAGE;
#pragma unroll
      for (int ks = 0; ks < 2; ++ks) {
        bf16x8 a[2], b[2];
#pragma unroll
        for (int mi = 0; mi < 2; ++mi) a[mi] = *reinterpret_cast<const bf16x8*>(base + A_OFF(wm * 32 + mi * 16 + l15, ks * 4 + lg));
#pragma unroll
        for (int ni = 0; ni < 2; ++ni) {
          const int ch = wn * 4 + ni * 2 + (lp >> 1);
          const int kr = ks * 32 + lg * 8 + lq;
          const s16x4 lo = __builtin_amdgcn_ds_read_tr16_b64_v4i16((lds_s16x4*)(base + 16384 + B3_OFF(kr, ch) + 8 * (lp & 1)));
          const s16x4 hi = __builtin_amdgcn_ds_read_tr16_b64_v4i16((lds_s16x4*)(base + 16384 + B3_OFF(kr + 4, ch) + 8 * (lp & 1)));
          b[ni] = __builtin_shufflevector(lo, hi, 0, 1, 2, 3, 4, 5, 6, 7);
        }
#pragma unroll
        for (int mi = 0; mi < 2; ++mi)
#pragma unroll
          for (int ni = 0; ni < 2; ++ni)
            acc[mi][ni] = __builtin_amdgcn_mfma_f32_16x16x32_bf16(b[ni], a[mi], acc[mi][ni], 0, 0, 0);
      }
    }
    if (T + 1 < 8) swrite(T + 1);
    __syncthreads();
  }
  const float* mf = reinterpret_cast<const float*>(ws + WS_MODF);
  const float* wr1 = reinterpret_cast<const float*>(ws + WS_WR1);
  u16* x1 = reinterpret_cast<u16*>(ws + WS_X1);
  float* rs = reinterpret_cast<float*>(ws + WS_RS2);
#pragma unroll
  for (int mi = 0; mi < 2; ++mi) {
    const int m = row0 + wm * 32 + mi * 16 + l15;
    float s1 = 0.f, s2 = 0.f, s3 = 0.f;
#pragma unroll
    for (int ni = 0; ni < 2; ++ni) {
      const int n = col0 + wn * 32 + ni * 16 + lg * 4;
      const u32x2 xv = *reinterpret_cast<const u32x2*>(x + (size_t)m * D_ + n);
      const f32x4 g = *reinterpret_cast<const f32x4*>(mf + 2048 + n);
      const f32x4 w = *reinterpret_cast<const f32x4*>(wr1 + n);
      float v[4];
      v[0] = rbf(lo2f(xv[0]) + rbf(g[0] * rbf(acc[mi][ni][0])));
      v[1] = rbf(hi2f(xv[0]) + rbf(g[1] * rbf(acc[mi][ni][1])));
      v[2] = rbf(lo2f(xv[1]) + rbf(g[2] * rbf(acc[mi][ni][2])));
      v[3] = rbf(hi2f(xv[1]) + rbf(g[3] * rbf(acc[mi][ni][3])));
      u32x2 ov = {pk2(v[0], v[1]), pk2(v[2], v[3])};
      *reinterpret_cast<u32x2*>(x1 + (size_t)m * D_ + n) = ov;
#pragma unroll
      for (int j = 0; j < 4; ++j) { s1 += v[j]; s2 += v[j] * v[j]; s3 += v[j] * w[j]; }
    }
    s1 += __shfl_xor(s1, 16); s1 += __shfl_xor(s1, 32);
    s2 += __shfl_xor(s2, 16); s2 += __shfl_xor(s2, 32);
    s3 += __shfl_xor(s3, 16); s3 += __shfl_xor(s3, 32);
    if (lg == 0) { atomicAdd(rs + m, s1); atomicAdd(rs + 2048 + m, s2); atomicAdd(rs + 4096 + m, s3); }
  }
}

__device__ __forceinline__ void row_stats2(const float* rs, int row, float& m, float& rstd) {
  const float mean = rs[row] * (1.f / D_);
  float var = rs[2048 + row] * (1.f / D_) - mean * mean;
  var = fmaxf(var, 0.f);
  m = rbf(mean);
  rstd = rbf(sqrtf(rbf(rbf(var) + 1e-5f)));
}
__device__ __forceinline__ float ln2(float x, float m, float sd, float a, float s) {
  return rbf(rbf(rbf(rbf(x - m) / sd) * a) + s);
}

__global__ __launch_bounds__(256) void k_gateup(const u16* __restrict__ wg, const u16* __restrict__ wu,
                                                char* __restrict__ ws, u16* __restrict__ out) {
  const int tid = threadIdx.x, lane = tid & 63, wid = tid >> 6;
  const float* rs = reinterpret_cast<const float*>(ws + WS_RS2);
  const float* mf = reinterpret_cast<const float*>(ws + WS_MODF);
  const u16* x1 = reinterpret_cast<const u16*>(ws + WS_X1);
  if (blockIdx.x < 224) {
    __shared__ float sv[4][2];
    __shared__ int si[4][2];
    __shared__ float hs[2][256];
    __shared__ float red[4][4][64];
    const int cg = blockIdx.x % 56, ks = blockIdx.x / 56;
    const int c8 = tid & 7, kr = tid >> 3;
    float ra[8], rb[8], rc[8];
#pragma unroll
    for (int i = 0; i < 8; ++i) { ra[i] = rs[i * 256 + tid]; rb[i] = rs[2048 + i * 256 + tid]; rc[i] = rs[4096 + i * 256 + tid]; }
    const float W1 = reinterpret_cast<const float*>(ws + WS_W12)[0], W2 = reinterpret_cast<const float*>(ws + WS_W12)[1];
    const int kk = ks * 256 + tid;
    const float ma = mf[4096 + kk], ms = mf[3072 + kk];
    u32x4 g[8], u[8];
#pragma unroll
    for (int i = 0; i < 8; ++i) {
      const size_t off = (size_t)(ks * 256 + i * 32 + kr) * F_ + cg * 64 + c8 * 8;
      g[i] = *reinterpret_cast<const u32x4*>(wg + off);
      u[i] = *reinterpret_cast<const u32x4*>(wu + off);
    }
    __builtin_amdgcn_sched_barrier(0);
    float v1 = -3.0e38f, v2 = -3.0e38f; int i1 = 0x7fffffff, i2 = 0x7fffffff;
#pragma unroll
    for (int i = 0; i < 8; ++i) {
      const int row = i * 256 + tid;
      const float mean = ra[i] * (1.f / D_);
      const float var = fmaxf(rb[i] * (1.f / D_) - mean * mean, 0.f);
      const float m = rbf(mean), rstd = 1.f / rbf(sqrtf(rbf(rbf(var) + 1e-5f)));
      const float r = rbf(rstd * (rc[i] - m * W1) + W2);
      const bool b1 = r > v1, b2 = r > v2;
      v2 = b1 ? v1 : (b2 ? r : v2); i2 = b1 ? i1 : (b2 ? row : i2);
      v1 = b1 ? r : v1; i1 = b1 ? row : i1;
    }
#pragma unroll
    for (int o = 32; o > 0; o >>= 1) {
      const float a1 = __shfl_xor(v1, o), a2 = __shfl_xor(v2, o);
      const int b1 = __shfl_xor(i1, o), b2 = __shfl_xor(i2, o);
      if (a1 > v1 || (a1 == v1 && b1 < i1)) {
        if (v1 > a2 || (v1 == a2 && i1 < b2)) { v2 = v1; i2 = i1; } else { v2 = a2; i2 = b2; }
        v1 = a1; i1 = b1;
      } else if (a1 > v2 || (a1 == v2 && b1 < i2)) { v2 = a1; i2 = b1; }
    }
    if (lane == 0) { sv[wid][0] = v1; sv[wid][1] = v2; si[wid][0] = i1; si[wid][1] = i2; }
    __syncthreads();
    v1 = sv[0][0]; v2 = sv[0][1]; i1 = si[0][0]; i2 = si[0][1];
#pragma unroll
    for (int w = 1; w < 4; ++w) {
      const float a1 = sv[w][0], a2 = sv[w][1]; const int b1 = si[w][0], b2 = si[w][1];
      if (a1 > v1 || (a1 == v1 && b1 < i1)) {
        if (v1 > a2 || (v1 == a2 && i1 < b2)) { v2 = v1; i2 = i1; } else { v2 = a2; i2 = b2; }
        v1 = a1; i1 = b1;
      } else if (a1 > v2 || (a1 == v2 && b1 < i2)) { v2 = a1; i2 = b1; }
    }
    if (blockIdx.x == 0 && tid == 0) {
      int* sel = reinterpret_cast<int*>(ws + WS_SEL);
      sel[0] = i1; sel[1] = i2;
      reinterpret_cast<float*>(sel)[2] = v1; reinterpret_cast<float*>(sel)[3] = v2;
    }
    {
      const int k = kk;
      const float a = rbf(1.f + ma), s = ms;
      float m, rstd;
      row_stats2(rs, i1, m, rstd);
      hs[0][tid] = ln2(bf2f(x1[(size_t)i1 * D_ + k]), m, rstd, a, s);
      row_stats2(rs, i2, m, rstd);
      hs[1][tid] = ln2(bf2f(x1[(size_t)i2 * D_ + k]), m, rstd, a, s);
    }
    __syncthreads();
    float ag[2][8], au[2][8];
#pragma unroll
    for (int r = 0; r < 2; ++r)
#pragma unroll
      for (int j = 0; j < 8; ++j) { ag[r][j] = 0.f; au[r][j] = 0.f; }
#pragma unroll
    for (int i = 0; i < 8; ++i) {
      const float h0 = hs[0][i * 32 + kr], h1 = hs[1][i * 32 + kr];
#pragma unroll
      for (int j = 0; j < 4; ++j) {
        ag[0][2 * j] += h0 * lo2f(g[i][j]); ag[0][2 * j + 1] += h0 * hi2f(g[i][j]);
        ag[1][2 * j] += h1 * lo2f(g[i][j]); ag[1][2 * j + 1] += h1 * hi2f(g[i][j]);
        au[0][2 * j] += h0 * lo2f(u[i][j]); au[0][2 * j + 1] += h0 * hi2f(u[i][j]);
        au[1][2 * j] += h1 * lo2f(u[i][j]); au[1][2 * j + 1] += h1 * hi2f(u[i][j]);
      }
    }
#pragma unroll
    for (int r = 0; r < 2; ++r)
#pragma unroll
      for (int j = 0; j < 8; ++j) {
        float a = ag[r][j], b = au[r][j];
        a += __shfl_xor(a, 8); a += __shfl_xor(a, 16); a += __shfl_xor(a, 32);
        b += __shfl_xor(b, 8); b += __shfl_xor(b, 16); b += __shfl_xor(b, 32);
        if (lane < 8) { red[wid][r][lane * 8 + j] = a; red[wid][2 + r][lane * 8 + j] = b; }
      }
    __syncthreads();
    {
      const int q = tid >> 6, cc = tid & 63;
      const float v = red[0][q][cc] + red[1][q][cc] + red[2][q][cc] + red[3][q][cc];
      reinterpret_cast<float*>(ws + WS_GUP)[(size_t)(ks * 4 + q) * F_ + cg * 64 + cc] = v;
    }
  } else {
    const int rbase = (blockIdx.x - 224) * 8;
#pragma unroll 1
    for (int rr = wid; rr < 8; rr += 4) {
      const int row = rbase + rr;
      float m, rstd; row_stats2(rs, row, m, rstd);
#pragma unroll
      for (int half = 0; half < 2; ++half) {
        const int k = half * 512 + lane * 8;
        const u32x4 xv = *reinterpret_cast<const u32x4*>(x1 + (size_t)row * D_ + k);
        float a[8], s[8], gb[8];
        *reinterpret_cast<f32x4*>(a) = *reinterpret_cast<const f32x4*>(mf + 4096 + k);
        *reinterpret_cast<f32x4*>(a + 4) = *reinterpret_cast<const f32x4*>(mf + 4096 + k + 4);
        *reinterpret_cast<f32x4*>(s) = *reinterpret_cast<const f32x4*>(mf + 3072 + k);
        *reinterpret_cast<f32x4*>(s + 4) = *reinterpret_cast<const f32x4*>(mf + 3072 + k + 4);
        *reinterpret_cast<f32x4*>(gb) = *reinterpret_cast<const f32x4*>(mf + 5120 + k);
        *reinterpret_cast<f32x4*>(gb + 4) = *reinterpret_cast<const f32x4*>(mf + 5120 + k + 4);
        u32x4 ov;
#pragma unroll
        for (int j = 0; j < 4; ++j) {
          const float xa = lo2f(xv[j]), xb = hi2f(xv[j]);
          const float ha = ln2(xa, m, rstd, rbf(1.f + a[2 * j]), s[2 * j]);
          const float hb = ln2(xb, m, rstd, rbf(1.f + a[2 * j + 1]), s[2 * j + 1]);
          ov[j] = pk2(xa + rbf(gb[2 * j] * ha), xb + rbf(gb[2 * j + 1] * hb));
        }
        *reinterpret_cast<u32x4*>(out + (size_t)row * D_ + k) = ov;
      }
    }
  }
}

__global__ __launch_bounds__(256) void k_down(const u16* __restrict__ wd, char* __restrict__ ws, u16* __restrict__ out) {
  __shared__ float gs[2][224];
  __shared__ float red[4][2][64];
  __shared__ int last;
  const int tid = threadIdx.x, lane = tid & 63, wid = tid >> 6;
  const int cg = blockIdx.x & 15, ks = blockIdx.x >> 4;
  const int c8 = tid & 7, kr = tid >> 3;
  u32x4 w[7];
#pragma unroll
  for (int i = 0; i < 7; ++i)
    w[i] = *reinterpret_cast<const u32x4*>(wd + (size_t)(ks * 224 + i * 32 + kr) * D_ + cg * 64 + c8 * 8);
  const float* gup = reinterpret_cast<const float*>(ws + WS_GUP);
  if (tid < 224) {
    const int f = ks * 224 + tid;
#pragma unroll
    for (int r = 0; r < 2; ++r) {
      float g = 0.f, u = 0.f;
#pragma unroll
      for (int p = 0; p < 4; ++p) { g += gup[(size_t)(p * 4 + r) * F_ + f]; u += gup[(size_t)(p * 4 + 2 + r) * F_ + f]; }
      gs[r][tid] = rbf(rbf(silu_f(rbf(g))) * rbf(u));
    }
  }
  __syncthreads();
  float a0[8], a1[8];
#pragma unroll
  for (int j = 0; j < 8; ++j) { a0[j] = 0.f; a1[j] = 0.f; }
#pragma unroll
  for (int i = 0; i < 7; ++i) {
    const float g0 = gs[0][i * 32 + kr], g1 = gs[1][i * 32 + kr];
#pragma unroll
    for (int j = 0; j < 4; ++j) {
      a0[2 * j] += g0 * lo2f(w[i][j]); a0[2 * j + 1] += g0 * hi2f(w[i][j]);
      a1[2 * j] += g1 * lo2f(w[i][j]); a1[2 * j + 1] += g1 * hi2f(w[i][j]);
    }
  }
#pragma unroll
  for (int j = 0; j < 8; ++j) {
    float a = a0[j], b = a1[j];
    a += __shfl_xor(a, 8); a += __shfl_xor(a, 16); a += __shfl_xor(a, 32);
    b += __shfl_xor(b, 8); b += __shfl_xor(b, 16); b += __shfl_xor(b, 32);
    if (lane < 8) { red[wid][0][lane * 8 + j] = a; red[wid][1][lane * 8 + j] = b; }
  }
  __syncthreads();
  float* yp = reinterpret_cast<float*>(ws + WS_YP);
  if (tid < 128) {
    const int r = tid >> 6, cc = tid & 63;
    atomicAdd(yp + (size_t)r * D_ + cg * 64 + cc, red[0][r][cc] + red[1][r][cc] + red[2][r][cc] + red[3][r][cc]);
  }
  asm volatile("s_waitcnt vmcnt(0)" ::: "memory");
  __syncthreads();
  if (tid == 0) {
    const unsigned t = atomicAdd(reinterpret_cast<unsigned*>(ws + WS_CNT) + cg, 1u);
    last = (t == 15u);
  }
  __syncthreads();
  if (last) {
    if (tid < 128) {
      const int r = tid >> 6, cc = tid & 63, n = cg * 64 + cc;
      float y = __hip_atomic_load(yp + (size_t)r * D_ + n, __ATOMIC_RELAXED, __HIP_MEMORY_SCOPE_AGENT);
      const int* sel = reinterpret_cast<const int*>(ws + WS_SEL);
      const int row = sel[r];
      const float rv = reinterpret_cast<const float*>(sel)[2 + r];
      const float sg = rbf(1.f / (1.f + __expf(-rv)));
      y = rbf(rbf(y) * sg);
      const float* rs = reinterpret_cast<const float*>(ws + WS_RS2);
      const float* mf = reinterpret_cast<const float*>(ws + WS_MODF);
      const u16* x1 = reinterpret_cast<const u16*>(ws + WS_X1);
      float m, rstd; row_stats2(rs, row, m, rstd);
      const float xv = bf2f(x1[(size_t)row * D_ + n]);
      const float h2 = ln2(xv, m, rstd, rbf(1.f + mf[4096 + n]), mf[3072 + n]);
      const float B = rbf(h2 + y);
      out[(size_t)row * D_ + n] = (u16)(pk2(xv + rbf(mf[5120 + n] * B), 0.f) & 0xffff);
    }
  }
}

extern "C" void kernel_launch(void* const* d_in, const int* in_sizes, int n_in, void* d_out, int out_size,
                              void* d_ws, size_t ws_size, hipStream_t stream) {
  const u16* x = (const u16*)d_in[0];
  const u16* wqkv = (const u16*)d_in[1];
  const u16* wphi = (const u16*)d_in[2];
  const u16* wo = (const u16*)d_in[3];
  const u16* wr = (const u16*)d_in[5];
  const u16* wg = (const u16*)d_in[6];
  const u16* wu = (const u16*)d_in[7];
  const u16* wd = (const u16*)d_in[8];
  const u16* c = (const u16*)d_in[9];
  const u16* wmod = (const u16*)d_in[10];
  char* ws = (char*)d_ws;
  u16* out = (u16*)d_out;
  hipFuncSetAttribute((const void*)k_qkv, hipFuncAttributeMaxDynamicSharedMemorySize, K1_LDS);
  k_prep<<<896, 256, 0, stream>>>(x, c, wmod, wqkv, wo, ws);
  k_qkv<<<256, 512, K1_LDS, stream>>>(x, wqkv, wphi, ws);
  k_o<<<257, 256, 0, stream>>>(wphi, wr, ws);
  k_wo<<<256, 512, 0, stream>>>(x, wo, ws);
  k_gateup<<<480, 256, 0, stream>>>(wg, wu, ws, out);
  k_down<<<256, 256, 0, stream>>>(wd, ws, out);
}
```

```cpp
#include <hip/hip_runtime.h>
#include <stdint.h>

typedef unsigned short u16;
typedef short bf16x8 __attribute__((ext_vector_type(8)));
typedef short s16x4 __attribute__((ext_vector_type(4)));
typedef float f32x2 __attribute__((ext_vector_type(2)));
typedef float f32x4 __attribute__((ext_vector_type(4)));
typedef float f32x16 __attribute__((ext_vector_type(16)));
typedef unsigned u32x4 __attribute__((ext_vector_type(4)));
typedef unsigned u32x2 __attribute__((ext_vector_type(2)));
typedef __bf16 bf16x2v __attribute__((ext_vector_type(2)));
typedef __attribute__((address_space(3))) s16x4 lds_s16x4;

#define S_ 2048
#define D_ 1024
#define H_ 32
#define F_ 3584

#define WS_MODP   0u
#define WS_STATS  (WS_MODP + 4u*6144u*4u)
#define WS_RS2    (WS_STATS + 2048u*8u)
#define WS_CNT    (WS_RS2 + 3u*2048u*4u)
#define WS_MODF   (WS_CNT + 256u)
#define WS_WR1    (WS_MODF + 6144u*4u)
#define WS_W12    (WS_WR1 + 1024u*4u)
#define WS_SEL    (WS_W12 + 64u)
#define WS_GUP    (WS_SEL + 64u)
#define WS_YP     (WS_GUP + 4u*2u*2u*3584u*4u)
#define WS_Q      (WS_YP + 2u*1024u*4u)
#define WS_O      (WS_Q + 2048u*1024u*2u)
#define WS_X1     (WS_O + 2048u*1024u*2u)
#define WS_KVP    (WS_X1 + 2048u*1024u*2u)
#define WS_WQT    (WS_KVP + 16u*32u*2112u*4u)
#define WS_WOT    (WS_WQT + 1024u*3072u*2u)
#define WS_XN     (WS_WOT + 1024u*1024u*2u)
#define WS_HB     (WS_XN + 2048u*1024u*2u)
#define WS_END    (WS_HB + 2048u*1024u*2u)

__device__ __forceinline__ float bf2f(u16 u) { return __uint_as_float(((unsigned)u) << 16); }
__device__ __forceinline__ unsigned pk2(float a, float b) {
  f32x2 f = {a, b}; bf16x2v h = __builtin_convertvector(f, bf16x2v); return __builtin_bit_cast(unsigned, h);
}
__device__ __forceinline__ float rbf(float a) {
  return __uint_as_float(pk2(a, 0.f) << 16);
}
__device__ __forceinline__ float lo2f(unsigned w) { return __uint_as_float(w << 16); }
__device__ __forceinline__ float hi2f(unsigned w) { return __uint_as_float(w & 0xffff0000u); }
__device__ __forceinline__ float wave_sum(float v) {
  for (int o = 32; o > 0; o >>= 1) v += __shfl_xor(v, o);
  return v;
}
__device__ __forceinline__ float silu_f(float x) { return x / (1.f + __expf(-x)); }

__global__ __launch_bounds__(256) void k_prep(const u16* __restrict__ x, const u16* __restrict__ c,
                                              const u16* __restrict__ wmod, const u16* __restrict__ wqkv,
                                              const u16* __restrict__ wo, char* __restrict__ ws) {
  const int tid = threadIdx.x, lane = tid & 63, wid = tid >> 6;
  if (blockIdx.x < 384) {
    __shared__ float red[4][64];
    const int cg = blockIdx.x % 96, ks = blockIdx.x / 96;
    const int c8 = tid & 7, kr = tid >> 3;
    u32x4 w[8];
    float sc[8];
#pragma unroll
    for (int i = 0; i < 8; ++i) {
      const int k = ks * 256 + i * 32 + kr;
      w[i] = *reinterpret_cast<const u32x4*>(wmod + (size_t)k * 6144 + cg * 64 + c8 * 8);
      sc[i] = rbf(silu_f(bf2f(c[k])));
    }
    float acc[8] = {0, 0, 0, 0, 0, 0, 0, 0};
#pragma unroll
    for (int i = 0; i < 8; ++i)
#pragma unroll
      for (int j = 0; j < 4; ++j) { acc[2 * j] += sc[i] * lo2f(w[i][j]); acc[2 * j + 1] += sc[i] * hi2f(w[i][j]); }
#pragma unroll
    for (int j = 0; j < 8; ++j) {
      float v = acc[j];
      v += __shfl_xor(v, 8); v += __shfl_xor(v, 16); v += __shfl_xor(v, 32);
      acc[j] = v;
    }
    if (lane < 8)
#pragma unroll
      for (int j = 0; j < 8; ++j) red[wid][lane * 8 + j] = acc[j];
    __syncthreads();
    if (tid < 64) {
      float v = red[0][tid] + red[1][tid] + red[2][tid] + red[3][tid];
      reinterpret_cast<float*>(ws + WS_MODP)[ks * 6144 + cg * 64 + tid] = v;
    }
  } else {
    const int row = (blockIdx.x - 384) * 4 + wid;
    const u32x4 a = *reinterpret_cast<const u32x4*>(x + (size_t)row * D_ + lane * 8);
    const u32x4 b = *reinterpret_cast<const u32x4*>(x + (size_t)row * D_ + 512 + lane * 8);
    float s = 0.f;
#pragma unroll
    for (int j = 0; j < 4; ++j) s += lo2f(a[j]) + hi2f(a[j]) + lo2f(b[j]) + hi2f(b[j]);
    const float mean = wave_sum(s) * (1.f / D_);
    float q = 0.f;
#pragma unroll
    for (int j = 0; j < 4; ++j) {
      float d;
      d = lo2f(a[j]) - mean; q += d * d; d = hi2f(a[j]) - mean; q += d * d;
      d = lo2f(b[j]) - mean; q += d * d; d = hi2f(b[j]) - mean; q += d * d;
    }
    const float var = wave_sum(q) * (1.f / D_);
    {
      const float sd = rbf(sqrtf(rbf(rbf(var) + 1e-5f))), mb = rbf(mean);
      u32x4 oa, ob;
#pragma unroll
      for (int j = 0; j < 4; ++j) {
        oa[j] = pk2(rbf(lo2f(a[j]) - mb) / sd, rbf(hi2f(a[j]) - mb) / sd);
        ob[j] = pk2(rbf(lo2f(b[j]) - mb) / sd, rbf(hi2f(b[j]) - mb) / sd);
      }
      u16* xn = reinterpret_cast<u16*>(ws + WS_XN);
      *reinterpret_cast<u32x4*>(xn + (size_t)row * D_ + lane * 8) = oa;
      *reinterpret_cast<u32x4*>(xn + (size_t)row * D_ + 512 + lane * 8) = ob;
    }
    if (lane == 0) {
      float* rs = reinterpret_cast<float*>(ws + WS_RS2);
      rs[row] = 0.f; rs[2048 + row] = 0.f; rs[4096 + row] = 0.f;
    }
    if (blockIdx.x == 384 && tid < 64) reinterpret_cast<unsigned*>(ws + WS_CNT)[tid] = 0u;
    {
      const int bi = blockIdx.x - 384;
      if (bi < 2) reinterpret_cast<f32x4*>(ws + WS_YP)[bi * 256 + tid] = f32x4{0.f, 0.f, 0.f, 0.f};
    }
  }
}

#define A_OFF(row, ch) ((row) * 128 + ((((ch) ^ ((row) & 7))) << 4))
#define B_OFF(row, ch) ((((row) >> 3) * 6 + ((ch) >> 2)) * 512 + ((row) & 7) * 64 + ((((ch) & 3) ^ (((row) >> 2) & 3)) << 4))
#define K1_STAGE 40960
#define K1_MOD (92160)
#define K1_WPHI (K1_MOD + 8192)
#define K1_LDS (K1_WPHI + 4096)
#define C_STRIDE 400
#define PHI_OFF (128 * C_STRIDE)
#define PHI_STRIDE 144

__global__ __launch_bounds__(512) void k_qkv(const u16* __restrict__ x, const u16* __restrict__ wqkv,
                                             const u16* __restrict__ wphi, char* __restrict__ ws) {
  extern __shared__ __attribute__((aligned(16))) char smem[];
  const int tid = threadIdx.x, lane = tid & 63, wid = tid >> 6;
  const int xcd = blockIdx.x & 7, idx = blockIdx.x >> 3;
  const int mt = (xcd & 1) * 8 + (idx & 7), hp = (xcd >> 1) * 4 + (idx >> 3);
  const int row0 = mt * 128;
  float* lmod = reinterpret_cast<float*>(smem + K1_MOD);
  const int lg = lane >> 4, l15 = lane & 15, lq = (lane & 15) >> 2, lp = lane & 3;
  const int sr = tid >> 3, sch = tid & 7;
  const int wm = wid & 1, wn = wid >> 1;
  const u16* xa0 = reinterpret_cast<const u16*>(ws + WS_XN) + (size_t)(row0 + sr) * D_ + sch * 8;
  const u16* xa1 = xa0 + 64 * D_;
  const u16* wb = wqkv + (size_t)sr * 3072 + hp * 64 + sch * 8;
  u32x4 rg[4][5];
  auto gload = [&](int t) {
    u32x4* g = rg[t & 3];
    g[0] = *reinterpret_cast<const u32x4*>(xa0 + t * 64);
    g[1] = *reinterpret_cast<const u32x4*>(xa1 + t * 64);
    const u16* p = wb + (size_t)t * 64 * 3072;
    g[2] = *reinterpret_cast<const u32x4*>(p);
    g[3] = *reinterpret_cast<const u32x4*>(p + 1024);
    g[4] = *reinterpret_cast<const u32x4*>(p + 2048);
  };
  auto swrite = [&](int t, int buf) {
    char* base = smem + buf * K1_STAGE;
    const u32x4 ga0 = rg[t & 3][0], ga1 = rg[t & 3][1];
    const f32x4 a0 = *reinterpret_cast<const f32x4*>(lmod + t * 64 + sch * 8);
    const f32x4 a1 = *reinterpret_cast<const f32x4*>(lmod + t * 64 + sch * 8 + 4);
    const f32x4 s0 = *reinterpret_cast<const f32x4*>(lmod + 1024 + t * 64 + sch * 8);
    const f32x4 s1 = *reinterpret_cast<const f32x4*>(lmod + 1024 + t * 64 + sch * 8 + 4);
    const float av[8] = {a0[0], a0[1], a0[2], a0[3], a1[0], a1[1], a1[2], a1[3]};
    const float sv[8] = {s0[0], s0[1], s0[2], s0[3], s1[0], s1[1], s1[2], s1[3]};
    u32x4 h0, h1;
#pragma unroll
    for (int j = 0; j < 4; ++j) {
      const unsigned t0 = pk2(lo2f(ga0[j]) * av[2 * j], hi2f(ga0[j]) * av[2 * j + 1]);
      const unsigned t1 = pk2(lo2f(ga1[j]) * av[2 * j], hi2f(ga1[j]) * av[2 * j + 1]);
      h0[j] = pk2(lo2f(t0) + sv[2 * j], hi2f(t0) + sv[2 * j + 1]);
      h1[j] = pk2(lo2f(t1) + sv[2 * j], hi2f(t1) + sv[2 * j + 1]);
    }
    *reinterpret_cast<u32x4*>(base + A_OFF(sr, sch)) = h0;
    *reinterpret_cast<u32x4*>(base + A_OFF(sr + 64, sch)) = h1;
    *reinterpret_cast<u32x4*>(base + 16384 + B_OFF(sr, sch)) = rg[t & 3][2];
    *reinterpret_cast<u32x4*>(base + 16384 + B_OFF(sr, 8 + sch)) = rg[t & 3][3];
    *reinterpret_cast<u32x4*>(base + 16384 + B_OFF(sr, 16 + sch)) = rg[t & 3][4];
  };
  f32x4 acc[4][3];
#pragma unroll
  for (int i = 0; i < 4; ++i)
#pragma unroll
    for (int j = 0; j < 3; ++j) acc[i][j] = f32x4{0.f, 0.f, 0.f, 0.f};
  gload(0); gload(1); gload(2);
  {
    if (tid < 256) *reinterpret_cast<u32x4*>(smem + K1_WPHI + tid * 16) = *reinterpret_cast<const u32x4*>(wphi + (size_t)hp * 2048 + tid * 8);
    const float* mp = reinterpret_cast<const float*>(ws + WS_MODP);
#pragma unroll
    for (int i = tid; i < 1024; i += 512) {
      const float sa = mp[i] + mp[6144 + i] + mp[2 * 6144 + i] + mp[3 * 6144 + i];
      const float ca = mp[1024 + i] + mp[6144 + 1024 + i] + mp[2 * 6144 + 1024 + i] + mp[3 * 6144 + 1024 + i];
      lmod[i] = rbf(1.f + rbf(ca));
      lmod[1024 + i] = rbf(sa);
    }
  }
  __syncthreads();
  swrite(0, 0);
  __syncthreads();
#pragma unroll
  for (int t = 0; t < 16; ++t) {
    if (t < 13) gload(t + 3);
    const char* base = smem + (t & 1) * K1_STAGE;
#pragma unroll
    for (int ks = 0; ks < 2; ++ks) {
      bf16x8 a[4], b[3];
#pragma unroll
      for (int mi = 0; mi < 4; ++mi) a[mi] = *reinterpret_cast<const bf16x8*>(base + A_OFF(wm * 64 + mi * 16 + l15, ks * 4 + lg));
#pragma unroll
      for (int ni = 0; ni < 3; ++ni) {
        const int ch = wn * 6 + ni * 2 + (lp >> 1);
        const int kr = ks * 32 + lg * 8 + lq;
        const s16x4 lo = __builtin_amdgcn_ds_read_tr16_b64_v4i16((lds_s16x4*)(base + 16384 + B_OFF(kr, ch) + 8 * (lp & 1)));
        const s16x4 hi = __builtin_amdgcn_ds_read_tr16_b64_v4i16((lds_s16x4*)(base + 16384 + B_OFF(kr + 4, ch) + 8 * (lp & 1)));
        b[ni] = __builtin_shufflevector(lo, hi, 0, 1, 2, 3, 4, 5, 6, 7);
      }
#pragma unroll
      for (int mi = 0; mi < 4; ++mi)
#pragma unroll
        for (int ni = 0; ni < 3; ++ni)
          acc[mi][ni] = __builtin_amdgcn_mfma_f32_16x16x32_bf16(b[ni], a[mi], acc[mi][ni], 0, 0, 0);
    }
    if (t < 15) swrite(t + 1, (t + 1) & 1);
    __syncthreads();
  }
#pragma unroll
  for (int mi = 0; mi < 4; ++mi)
#pragma unroll
    for (int ni = 0; ni < 3; ++ni) {
      const int m = wm * 64 + mi * 16 + l15, n = wn * 48 + ni * 16 + lg * 4;
      u32x2 v = {pk2(acc[mi][ni][0], acc[mi][ni][1]), pk2(acc[mi][ni][2], acc[mi][ni][3])};
      *reinterpret_cast<u32x2*>(smem + m * C_STRIDE + n * 2) = v;
    }
  __syncthreads();
  {
    u16* q = reinterpret_cast<u16*>(ws + WS_Q);
#pragma unroll
    for (int i = 0; i < 2; ++i) {
      const int r = sr + i * 64;
      const u32x4 v = *reinterpret_cast<const u32x4*>(smem + r * C_STRIDE + sch * 16);
      *reinterpret_cast<u32x4*>(q + (size_t)(row0 + r) * D_ + hp * 64 + sch * 8) = v;
    }
  }
  const int l31 = lane & 31, lh = lane >> 5;
  {
    const int hh = wid & 1, rt = wid >> 1;
    const u16* wp = reinterpret_cast<const u16*>(smem + K1_WPHI) + hh * 1024;
    f32x16 xp;
#pragma unroll
    for (int i = 0; i < 16; ++i) xp[i] = 0.f;
#pragma unroll
    for (int s = 0; s < 2; ++s) {
      bf16x8 a;
#pragma unroll
      for (int j = 0; j < 8; ++j) a[j] = (short)wp[(16 * s + 8 * lh + j) * 32 + l31];
      const bf16x8 b = *reinterpret_cast<const bf16x8*>(smem + (rt * 32 + l31) * C_STRIDE + (64 + hh * 32 + 16 * s + 8 * lh) * 2);
      xp = __builtin_amdgcn_mfma_f32_32x32x16_bf16(a, b, xp, 0, 0, 0);
    }
    float mx = -3.0e38f, mn = 3.0e38f;
#pragma unroll
    for (int i = 0; i < 16; ++i) { xp[i] = rbf(xp[i]); mx = fmaxf(mx, xp[i]); mn = fminf(mn, xp[i]); }
    mx = fmaxf(mx, __shfl_xor(mx, 32)); mn = fminf(mn, __shfl_xor(mn, 32));
    float ep[16], en[16], sp = 0.f, sn = 0.f;
#pragma unroll
    for (int i = 0; i < 16; ++i) { ep[i] = __expf(xp[i] - mx); sp += ep[i]; en[i] = __expf(mn - xp[i]); sn += en[i]; }
    sp += __shfl_xor(sp, 32); sn += __shfl_xor(sn, 32);
    const float ip = 1.f / sp, in_ = 1.f / sn;
    char* ph = smem + PHI_OFF + (hh * 128 + rt * 32 + l31) * PHI_STRIDE;
#pragma unroll
    for (int g = 0; g < 4; ++g) {
      u32x2 v = {pk2(ep[4 * g] * ip, ep[4 * g + 1] * ip), pk2(ep[4 * g + 2] * ip, ep[4 * g + 3] * ip)};
      *reinterpret_cast<u32x2*>(ph + (8 * g + 4 * lh) * 2) = v;
      u32x2 w = {pk2(en[4 * g] * in_, en[4 * g + 1] * in_), pk2(en[4 * g + 2] * in_, en[4 * g + 3] * in_)};
      *reinterpret_cast<u32x2*>(ph + 64 + (8 * g + 4 * lh) * 2) = w;
    }
  }
  __syncthreads();
  if (wid < 4) {
    const int hh = wid & 1, ft = wid >> 1;
    const int blk = (lane >> 4) & 1;
    f32x16 kv, zz;
#pragma unroll
    for (int i = 0; i < 16; ++i) { kv[i] = 0.f; zz[i] = 0.f; }
    bf16x8 ones;
#pragma unroll
    for (int j = 0; j < 8; ++j) ones[j] = (short)0x3f80;
#pragma unroll
    for (int s = 0; s < 8; ++s) {
      const char* pa = smem + PHI_OFF + (hh * 128 + 16 * s + 8 * lh + lq) * PHI_STRIDE + (ft * 32 + 16 * blk + 4 * lp) * 2;
      const s16x4 alo = __builtin_amdgcn_ds_read_tr16_b64_v4i16((lds_s16x4*)pa);
      const s16x4 ahi = __builtin_amdgcn_ds_read_tr16_b64_v4i16((lds_s16x4*)(pa + 4 * PHI_STRIDE));
      const bf16x8 a = __builtin_shufflevector(alo, ahi, 0, 1, 2, 3, 4, 5, 6, 7);
      const char* pb = smem + (16 * s + 8 * lh + lq) * C_STRIDE + (128 + hh * 32 + 16 * blk + 4 * lp) * 2;
      const s16x4 blo = __builtin_amdgcn_ds_read_tr16_b64_v4i16((lds_s16x4*)pb);
      const s16x4 bhi = __builtin_amdgcn_ds_read_tr16_b64_v4i16((lds_s16x4*)(pb + 4 * C_STRIDE));
      const bf16x8 b = __builtin_shufflevector(blo, bhi, 0, 1, 2, 3, 4, 5, 6, 7);
      kv = __builtin_amdgcn_mfma_f32_32x32x16_bf16(a, b, kv, 0, 0, 0);
      zz = __builtin_amdgcn_mfma_f32_32x32x16_bf16(a, ones, zz, 0, 0, 0);
    }
    float* kp = reinterpret_cast<float*>(ws + WS_KVP) + (size_t)((hp * 2 + hh) * 16 + mt) * 2112;
#pragma unroll
    for (int i = 0; i < 16; ++i) {
      const int f = ft * 32 + (i & 3) + 8 * (i >> 2) + 4 * lh;
      kp[f * 32 + l31] = kv[i];
      if (l31 == 0) kp[2048 + f] = zz[i];
    }
  }
}

__global__ __launch_bounds__(256) void k_o(const u16* __restrict__ wphi, const u16* __restrict__ wr, char* __restrict__ ws) {
  __shared__ __attribute__((aligned(16))) u16 kvt[32 * 68 + 64];
  __shared__ float zs[64];
  __shared__ float red[8];
  const int tid = threadIdx.x, lane = tid & 63, wid = tid >> 6;
  if (blockIdx.x == 256) {
    const float* mp = reinterpret_cast<const float*>(ws + WS_MODP);
    float* mf = reinterpret_cast<float*>(ws + WS_MODF);
    float* wr1 = reinterpret_cast<float*>(ws + WS_WR1);
    float s1 = 0.f, s2 = 0.f;
#pragma unroll
    for (int i = tid; i < 1024; i += 256) {
#pragma unroll
      for (int q = 0; q < 6; ++q)
        mf[q * 1024 + i] = rbf(mp[q * 1024 + i] + mp[6144 + q * 1024 + i] + mp[2 * 6144 + q * 1024 + i] + mp[3 * 6144 + q * 1024 + i]);
      const float sb = rbf(mp[3072 + i] + mp[6144 + 3072 + i] + mp[2 * 6144 + 3072 + i] + mp[3 * 6144 + 3072 + i]);
      const float cb = rbf(mp[4096 + i] + mp[6144 + 4096 + i] + mp[2 * 6144 + 4096 + i] + mp[3 * 6144 + 4096 + i]);
      const float w = bf2f(wr[i]);
      const float a = rbf(1.f + cb) * w;
      wr1[i] = a; s1 += a; s2 += sb * w;
    }
    s1 = wave_sum(s1); s2 = wave_sum(s2);
    if (lane == 0) { red[wid] = s1; red[4 + wid] = s2; }
    __syncthreads();
    if (tid == 0) {
      float* w12 = reinterpret_cast<float*>(ws + WS_W12);
      w12[0] = red[0] + red[1] + red[2] + red[3];
      w12[1] = red[4] + red[5] + red[6] + red[7];
    }
    return;
  }
  const int h = blockIdx.x & 31, rc = blockIdx.x >> 5;
  const int l31 = lane & 31, lh = lane >> 5;
  const u16* q = reinterpret_cast<const u16*>(ws + WS_Q);
  bf16x8 qb[2][2];
#pragma unroll
  for (int rt = 0; rt < 2; ++rt)
#pragma unroll
    for (int s = 0; s < 2; ++s)
      qb[rt][s] = *reinterpret_cast<const bf16x8*>(q + (size_t)(rc * 256 + wid * 64 + rt * 32 + l31) * D_ + h * 32 + 16 * s + 8 * lh);
  const u16* wp = wphi + (size_t)h * 1024;
  bf16x8 wa[2];
#pragma unroll
  for (int s = 0; s < 2; ++s)
#pragma unroll
    for (int j = 0; j < 8; ++j) wa[s][j] = (short)wp[(16 * s + 8 * lh + j) * 32 + l31];
  {
    const f32x4* kp = reinterpret_cast<const f32x4*>(ws + WS_KVP) + (size_t)h * 16 * 528;
    f32x4 s0 = {0.f, 0.f, 0.f, 0.f}, s1 = s0, s2 = s0;
#pragma unroll
    for (int m = 0; m < 16; ++m) {
      s0 += kp[m * 528 + tid];
      s1 += kp[m * 528 + 256 + tid];
    }
    if (wid == 0) {
#pragma unroll
      for (int m = 0; m < 16; ++m) s2 += kp[m * 528 + 512 + (lane & 15)];
    }
    {
      const int f = tid >> 3, d = (tid & 7) * 4;
#pragma unroll
      for (int j = 0; j < 4; ++j) {
        kvt[(d + j) * 68 + f] = (u16)(pk2(s0[j], 0.f) & 0xffff);
        kvt[(d + j) * 68 + 32 + f] = (u16)(pk2(s1[j], 0.f) & 0xffff);
      }
    }
    if (tid < 16)
#pragma unroll
      for (int j = 0; j < 4; ++j) zs[tid * 4 + j] = rbf(s2[j]);
  }
  __syncthreads();
  bf16x8 ka[4];
#pragma unroll
  for (int s = 0; s < 4; ++s) {
    const s16x4 lo = *reinterpret_cast<const s16x4*>(kvt + l31 * 68 + (s >> 1) * 32 + 16 * (s & 1) + 4 * lh);
    const s16x4 hi = *reinterpret_cast<const s16x4*>(kvt + l31 * 68 + (s >> 1) * 32 + 16 * (s & 1) + 8 + 4 * lh);
    ka[s] = __builtin_shufflevector(lo, hi, 0, 1, 2, 3, 4, 5, 6, 7);
  }
  float zp[16], zn[16];
#pragma unroll
  for (int i = 0; i < 16; ++i) { const int f = (i & 3) + 8 * (i >> 2) + 4 * lh; zp[i] = zs[f]; zn[i] = zs[32 + f]; }
  u16* o = reinterpret_cast<u16*>(ws + WS_O);
#pragma unroll
  for (int rt = 0; rt < 2; ++rt) {
    const int row = rc * 256 + wid * 64 + rt * 32 + l31;
    f32x16 xp;
#pragma unroll
    for (int i = 0; i < 16; ++i) xp[i] = 0.f;
#pragma unroll
    for (int s = 0; s < 2; ++s) {
      xp = __builtin_amdgcn_mfma_f32_32x32x16_bf16(wa[s], qb[rt][s], xp, 0, 0, 0);
    }
    float mx = -3.0e38f, mn = 3.0e38f;
#pragma unroll
    for (int i = 0; i < 16; ++i) { xp[i] = rbf(xp[i]); mx = fmaxf(mx, xp[i]); mn = fminf(mn, xp[i]); }
    mx = fmaxf(mx, __shfl_xor(mx, 32)); mn = fminf(mn, __shfl_xor(mn, 32));
    float ep[16], en[16], sp = 0.f, sn = 0.f;
#pragma unroll
    for (int i = 0; i < 16; ++i) { ep[i] = __expf(xp[i] - mx); sp += ep[i]; en[i] = __expf(mn - xp[i]); sn += en[i]; }
    sp += __shfl_xor(sp, 32); sn += __shfl_xor(sn, 32);
    const float ip = 1.f / sp, in_ = 1.f / sn;
    float den = 0.f;
    bf16x8 pb[4];
#pragma unroll
    for (int s = 0; s < 2; ++s) {
      u32x4 wpk, wnk;
#pragma unroll
      for (int j = 0; j < 4; ++j) {
        wpk[j] = pk2(ep[8 * s + 2 * j] * ip, ep[8 * s + 2 * j + 1] * ip);
        wnk[j] = pk2(en[8 * s + 2 * j] * in_, en[8 * s + 2 * j + 1] * in_);
        den += lo2f(wpk[j]) * zp[8 * s + 2 * j] + hi2f(wpk[j]) * zp[8 * s + 2 * j + 1];
        den += lo2f(wnk[j]) * zn[8 * s + 2 * j] + hi2f(wnk[j]) * zn[8 * s + 2 * j + 1];
      }
      pb[s] = __builtin_bit_cast(bf16x8, wpk);
      pb[2 + s] = __builtin_bit_cast(bf16x8, wnk);
    }
    den += __shfl_xor(den, 32);
    den = rbf(rbf(den) + 1e-6f);
    f32x16 num;
#pragma unroll
    for (int i = 0; i < 16; ++i) num[i] = 0.f;
#pragma unroll
    for (int s = 0; s < 4; ++s) num = __builtin_amdgcn_mfma_f32_32x32x16_bf16(ka[s], pb[s], num, 0, 0, 0);
#pragma unroll
    for (int g = 0; g < 4; ++g) {
      u32x2 v = {pk2(rbf(num[4 * g]) / den, rbf(num[4 * g + 1]) / den), pk2(rbf(num[4 * g + 2]) / den, rbf(num[4 * g + 3]) / den)};
      *reinterpret_cast<u32x2*>(o + (size_t)row * D_ + h * 32 + 8 * g + 4 * lh) = v;
    }
  }
}

#define B3_OFF(row, ch) ((((row) >> 3) * 2 + ((ch) >> 2)) * 512 + ((row) & 7) * 64 + ((((ch) & 3) ^ (((row) >> 2) & 3)) << 4))
#define K3_STAGE 24576
__global__ __launch_bounds__(512) void k_wo(const u16* __restrict__ x, const u16* __restrict__ wo, char* __restrict__ ws) {
  __shared__ __attribute__((aligned(16))) char smem[4 * K3_STAGE];
  const int tid = threadIdx.x, lane = tid & 63, wid = tid >> 6;
  const int xcd = blockIdx.x & 7, idx = blockIdx.x >> 3;
  const int mt = (xcd & 3) * 4 + (idx & 3), nt = (xcd >> 2) * 8 + (idx >> 2);
  const int row0 = mt * 128, col0 = nt * 64;
  const u16* o = reinterpret_cast<const u16*>(ws + WS_O);
  const int sr = tid >> 3, sch = tid & 7;
  const u16* pa0 = o + (size_t)(row0 + sr) * D_ + sch * 8;
  const u16* pa1 = pa0 + 64 * D_;
  const u16* pb = wo + (size_t)sr * D_ + col0 + sch * 8;
  u32x4 rg[3][6];
  auto gload = [&](int T) {
    u32x4* g = rg[T % 3];
#pragma unroll
    for (int hf = 0; hf < 2; ++hf) {
      const int t = T * 2 + hf;
      g[hf * 3 + 0] = *reinterpret_cast<const u32x4*>(pa0 + t * 64);
      g[hf * 3 + 1] = *reinterpret_cast<const u32x4*>(pa1 + t * 64);
      g[hf * 3 + 2] = *reinterpret_cast<const u32x4*>(pb + (size_t)t * 64 * D_);
    }
  };
  auto swrite = [&](int T) {
    const u32x4* g = rg[T % 3];
#pragma unroll
    for (int hf = 0; hf < 2; ++hf) {
      char* base = smem + ((T & 1) * 2 + hf) * K3_STAGE;
      *reinterpret_cast<u32x4*>(base + A_OFF(sr, sch)) = g[hf * 3 + 0];
      *reinterpret_cast<u32x4*>(base + A_OFF(sr + 64, sch)) = g[hf * 3 + 1];
      *reinterpret_cast<u32x4*>(base + 16384 + B3_OFF(sr, sch)) = g[hf * 3 + 2];
    }
  };
  const int wm = wid & 3, wn = wid >> 2;
  const int l15 = lane & 15, lg = lane >> 4, lq = (lane & 15) >> 2, lp = lane & 3;
  f32x4 acc[2][2];
#pragma unroll
  for (int i = 0; i < 2; ++i)
#pragma unroll
    for (int j = 0; j < 2; ++j) acc[i][j] = f32x4{0.f, 0.f, 0.f, 0.f};
  gload(0); gload(1);
  swrite(0);
  __syncthreads();
#pragma unroll
  for (int T = 0; T < 8; ++T) {
    if (T + 2 < 8) gload(T + 2);
#pragma unroll
    for (int hf = 0; hf < 2; ++hf) {
      const char* base = smem + ((T & 1) * 2 + hf) * K3_STAGE;
#pragma unroll
      for (int ks = 0; ks < 2; ++ks) {
        bf16x8 a[2], b[2];
#pragma unroll
        for (int mi = 0; mi < 2; ++mi) a[mi] = *reinterpret_cast<const bf16x8*>(base + A_OFF(wm * 32 + mi * 16 + l15, ks * 4 + lg));
#pragma unroll
        for (int ni = 0; ni < 2; ++ni) {
          const int ch = wn * 4 + ni * 2 + (lp >> 1);
          const int kr = ks * 32 + lg * 8 + lq;
          const s16x4 lo = __builtin_amdgcn_ds_read_tr16_b64_v4i16((lds_s16x4*)(base + 16384 + B3_OFF(kr, ch) + 8 * (lp & 1)));
          const s16x4 hi = __builtin_amdgcn_ds_read_tr16_b64_v4i16((lds_s16x4*)(base + 16384 + B3_OFF(kr + 4, ch) + 8 * (lp & 1)));
          b[ni] = __builtin_shufflevector(lo, hi, 0, 1, 2, 3, 4, 5, 6, 7);
        }
#pragma unroll
        for (int mi = 0; mi < 2; ++mi)
#pragma unroll
          for (int ni = 0; ni < 2; ++ni)
            acc[mi][ni] = __builtin_amdgcn_mfma_f32_16x16x32_bf16(b[ni], a[mi], acc[mi][ni], 0, 0, 0);
      }
    }
    if (T + 1 < 8) swrite(T + 1);
    __syncthreads();
  }
  const float* mf = reinterpret_cast<const float*>(ws + WS_MODF);
  const float* wr1 = reinterpret_cast<const float*>(ws + WS_WR1);
  u16* x1 = reinterpret_cast<u16*>(ws + WS_X1);
  float* rs = reinterpret_cast<float*>(ws + WS_RS2);
#pragma unroll
  for (int mi = 0; mi < 2; ++mi) {
    const int m = row0 + wm * 32 + mi * 16 + l15;
    float s1 = 0.f, s2 = 0.f, s3 = 0.f;
#pragma unroll
    for (int ni = 0; ni < 2; ++ni) {
      const int n = col0 + wn * 32 + ni * 16 + lg * 4;
      const u32x2 xv = *reinterpret_cast<const u32x2*>(x + (size_t)m * D_ + n);
      const f32x4 g = *reinterpret_cast<const f32x4*>(mf + 2048 + n);
      const f32x4 w = *reinterpret_cast<const f32x4*>(wr1 + n);
      float v[4];
      v[0] = rbf(lo2f(xv[0]) + rbf(g[0] * rbf(acc[mi][ni][0])));
      v[1] = rbf(hi2f(xv[0]) + rbf(g[1] * rbf(acc[mi][ni][1])));
      v[2] = rbf(lo2f(xv[1]) + rbf(g[2] * rbf(acc[mi][ni][2])));
      v[3] = rbf(hi2f(xv[1]) + rbf(g[3] * rbf(acc[mi][ni][3])));
      u32x2 ov = {pk2(v[0], v[1]), pk2(v[2], v[3])};
      *reinterpret_cast<u32x2*>(x1 + (size_t)m * D_ + n) = ov;
#pragma unroll
      for (int j = 0; j < 4; ++j) { s1 += v[j]; s2 += v[j] * v[j]; s3 += v[j] * w[j]; }
    }
    s1 += __shfl_xor(s1, 16); s1 += __shfl_xor(s1, 32);
    s2 += __shfl_xor(s2, 16); s2 += __shfl_xor(s2, 32);
    s3 += __shfl_xor(s3, 16); s3 += __shfl_xor(s3, 32);
    if (lg == 0) { atomicAdd(rs + m, s1); atomicAdd(rs + 2048 + m, s2); atomicAdd(rs + 4096 + m, s3); }
  }
}

__device__ __forceinline__ void row_stats2(const float* rs, int row, float& m, float& rstd) {
  const float mean = rs[row] * (1.f / D_);
  float var = rs[2048 + row] * (1.f / D_) - mean * mean;
  var = fmaxf(var, 0.f);
  m = rbf(mean);
  rstd = rbf(sqrtf(rbf(rbf(var) + 1e-5f)));
}
__device__ __forceinline__ float ln2(float x, float m, float sd, float a, float s) {
  return rbf(rbf(rbf(rbf(x - m) / sd) * a) + s);
}

__global__ __launch_bounds__(256) void k_gateup(const u16* __restrict__ wg, const u16* __restrict__ wu,
                                                char* __restrict__ ws, u16* __restrict__ out) {
  const int tid = threadIdx.x, lane = tid & 63, wid = tid >> 6;
  const float* rs = reinterpret_cast<const float*>(ws + WS_RS2);
  const float* mf = reinterpret_cast<const float*>(ws + WS_MODF);
  const u16* x1 = reinterpret_cast<const u16*>(ws + WS_X1);
  if (blockIdx.x < 224) {
    __shared__ float sv[4][2];
    __shared__ int si[4][2];
    __shared__ float hs[2][256];
    __shared__ float red[4][4][64];
    const int cg = blockIdx.x % 56, ks = blockIdx.x / 56;
    const int c8 = tid & 7, kr = tid >> 3;
    float ra[8], rb[8], rc[8];
#pragma unroll
    for (int i = 0; i < 8; ++i) { ra[i] = rs[i * 256 + tid]; rb[i] = rs[2048 + i * 256 + tid]; rc[i] = rs[4096 + i * 256 + tid]; }
    const float W1 = reinterpret_cast<const float*>(ws + WS_W12)[0], W2 = reinterpret_cast<const float*>(ws + WS_W12)[1];
    const int kk = ks * 256 + tid;
    const float ma = mf[4096 + kk], ms = mf[3072 + kk];
    u32x4 g[8], u[8];
#pragma unroll
    for (int i = 0; i < 8; ++i) {
      const size_t off = (size_t)(ks * 256 + i * 32 + kr) * F_ + cg * 64 + c8 * 8;
      g[i] = *reinterpret_cast<const u32x4*>(wg + off);
      u[i] = *reinterpret_cast<const u32x4*>(wu + off);
    }
    __builtin_amdgcn_sched_barrier(0);
    float v1 = -3.0e38f, v2 = -3.0e38f; int i1 = 0x7fffffff, i2 = 0x7fffffff;
#pragma unroll
    for (int i = 0; i < 8; ++i) {
      const int row = i * 256 + tid;
      const float mean = ra[i] * (1.f / D_);
      const float var = fmaxf(rb[i] * (1.f / D_) - mean * mean, 0.f);
      const float m = rbf(mean), rstd = 1.f / rbf(sqrtf(rbf(rbf(var) + 1e-5f)));
      const float r = rbf(rstd * (rc[i] - m * W1) + W2);
      const bool b1 = r > v1, b2 = r > v2;
      v2 = b1 ? v1 : (b2 ? r : v2); i2 = b1 ? i1 : (b2 ? row : i2);
      v1 = b1 ? r : v1; i1 = b1 ? row : i1;
    }
#pragma unroll
    for (int o = 32; o > 0; o >>= 1) {
      const float a1 = __shfl_xor(v1, o), a2 = __shfl_xor(v2, o);
      const int b1 = __shfl_xor(i1, o), b2 = __shfl_xor(i2, o);
      if (a1 > v1 || (a1 == v1 && b1 < i1)) {
        if (v1 > a2 || (v1 == a2 && i1 < b2)) { v2 = v1; i2 = i1; } else { v2 = a2; i2 = b2; }
        v1 = a1; i1 = b1;
      } else if (a1 > v2 || (a1 == v2 && b1 < i2)) { v2 = a1; i2 = b1; }
    }
    if (lane == 0) { sv[wid][0] = v1; sv[wid][1] = v2; si[wid][0] = i1; si[wid][1] = i2; }
    __syncthreads();
    v1 = sv[0][0]; v2 = sv[0][1]; i1 = si[0][0]; i2 = si[0][1];
#pragma unroll
    for (int w = 1; w < 4; ++w) {
      const float a1 = sv[w][0], a2 = sv[w][1]; const int b1 = si[w][0], b2 = si[w][1];
      if (a1 > v1 || (a1 == v1 && b1 < i1)) {
        if (v1 > a2 || (v1 == a2 && i1 < b2)) { v2 = v1; i2 = i1; } else { v2 = a2; i2 = b2; }
        v1 = a1; i1 = b1;
      } else if (a1 > v2 || (a1 == v2 && b1 < i2)) { v2 = a1; i2 = b1; }
    }
    if (blockIdx.x == 0 && tid == 0) {
      int* sel = reinterpret_cast<int*>(ws + WS_SEL);
      sel[0] = i1; sel[1] = i2;
      reinterpret_cast<float*>(sel)[2] = v1; reinterpret_cast<float*>(sel)[3] = v2;
    }
    {
      const int k = kk;
      const float a = rbf(1.f + ma), s = ms;
      float m, rstd;
      row_stats2(rs, i1, m, rstd);
      hs[0][tid] = ln2(bf2f(x1[(size_t)i1 * D_ + k]), m, rstd, a, s);
      row_stats2(rs, i2, m, rstd);
      hs[1][tid] = ln2(bf2f(x1[(size_t)i2 * D_ + k]), m, rstd, a, s);
    }
    __syncthreads();
    float ag[2][8], au[2][8];
#pragma unroll
    for (int r = 0; r < 2; ++r)
#pragma unroll
      for (int j = 0; j < 8; ++j) { ag[r][j] = 0.f; au[r][j] = 0.f; }
#pragma unroll
    for (int i = 0; i < 8; ++i) {
      const float h0 = hs[0][i * 32 + kr], h1 = hs[1][i * 32 + kr];
#pragma unroll
      for (int j = 0; j < 4; ++j) {
        ag[0][2 * j] += h0 * lo2f(g[i][j]); ag[0][2 * j + 1] += h0 * hi2f(g[i][j]);
        ag[1][2 * j] += h1 * lo2f(g[i][j]); ag[1][2 * j + 1] += h1 * hi2f(g[i][j]);
        au[0][2 * j] += h0 * lo2f(u[i][j]); au[0][2 * j + 1] += h0 * hi2f(u[i][j]);
        au[1][2 * j] += h1 * lo2f(u[i][j]); au[1][2 * j + 1] += h1 * hi2f(u[i][j]);
      }
    }
#pragma unroll
    for (int r = 0; r < 2; ++r)
#pragma unroll
      for (int j = 0; j < 8; ++j) {
        float a = ag[r][j], b = au[r][j];
        a += __shfl_xor(a, 8); a += __shfl_xor(a, 16); a += __shfl_xor(a, 32);
        b += __shfl_xor(b, 8); b += __shfl_xor(b, 16); b += __shfl_xor(b, 32);
        if (lane < 8) { red[wid][r][lane * 8 + j] = a; red[wid][2 + r][lane * 8 + j] = b; }
      }
    __syncthreads();
    {
      const int q = tid >> 6, cc = tid & 63;
      const float v = red[0][q][cc] + red[1][q][cc] + red[2][q][cc] + red[3][q][cc];
      reinterpret_cast<float*>(ws + WS_GUP)[(size_t)(ks * 4 + q) * F_ + cg * 64 + cc] = v;
    }
  } else {
    const int rbase = (blockIdx.x - 224) * 8;
#pragma unroll 1
    for (int rr = wid; rr < 8; rr += 4) {
      const int row = rbase + rr;
      float m, rstd; row_stats2(rs, row, m, rstd);
#pragma unroll
      for (int half = 0; half < 2; ++half) {
        const int k = half * 512 + lane * 8;
        const u32x4 xv = *reinterpret_cast<const u32x4*>(x1 + (size_t)row * D_ + k);
        float a[8], s[8], gb[8];
        *reinterpret_cast<f32x4*>(a) = *reinterpret_cast<const f32x4*>(mf + 4096 + k);
        *reinterpret_cast<f32x4*>(a + 4) = *reinterpret_cast<const f32x4*>(mf + 4096 + k + 4);
        *reinterpret_cast<f32x4*>(s) = *reinterpret_cast<const f32x4*>(mf + 3072 + k);
        *reinterpret_cast<f32x4*>(s + 4) = *reinterpret_cast<const f32x4*>(mf + 3072 + k + 4);
        *reinterpret_cast<f32x4*>(gb) = *reinterpret_cast<const f32x4*>(mf + 5120 + k);
        *reinterpret_cast<f32x4*>(gb + 4) = *reinterpret_cast<const f32x4*>(mf + 5120 + k + 4);
        u32x4 ov;
#pragma unroll
        for (int j = 0; j < 4; ++j) {
          const float xa = lo2f(xv[j]), xb = hi2f(xv[j]);
          const float ha = ln2(xa, m, rstd, rbf(1.f + a[2 * j]), s[2 * j]);
          const float hb = ln2(xb, m, rstd, rbf(1.f + a[2 * j + 1]), s[2 * j + 1]);
          ov[j] = pk2(xa + rbf(gb[2 * j] * ha), xb + rbf(gb[2 * j + 1] * hb));
        }
        *reinterpret_cast<u32x4*>(out + (size_t)row * D_ + k) = ov;
      }
    }
  }
}

__global__ __launch_bounds__(256) void k_down(const u16* __restrict__ wd, char* __restrict__ ws, u16* __restrict__ out) {
  __shared__ float gs[2][224];
  __shared__ float red[4][2][64];
  __shared__ int last;
  const int tid = threadIdx.x, lane = tid & 63, wid = tid >> 6;
  const int cg = blockIdx.x & 15, ks = blockIdx.x >> 4;
  const int c8 = tid & 7, kr = tid >> 3;
  u32x4 w[7];
#pragma unroll
  for (int i = 0; i < 7; ++i)
    w[i] = *reinterpret_cast<const u32x4*>(wd + (size_t)(ks * 224 + i * 32 + kr) * D_ + cg * 64 + c8 * 8);
  const float* gup = reinterpret_cast<const float*>(ws + WS_GUP);
  if (tid < 224) {
    const int f = ks * 224 + tid;
#pragma unroll
    for (int r = 0; r < 2; ++r) {
      float g = 0.f, u = 0.f;
#pragma unroll
      for (int p = 0; p < 4; ++p) { g += gup[(size_t)(p * 4 + r) * F_ + f]; u += gup[(size_t)(p * 4 + 2 + r) * F_ + f]; }
      gs[r][tid] = rbf(rbf(silu_f(rbf(g))) * rbf(u));
    }
  }
  __syncthreads();
  float a0[8], a1[8];
#pragma unroll
  for (int j = 0; j < 8; ++j) { a0[j] = 0.f; a1[j] = 0.f; }
#pragma unroll
  for (int i = 0; i < 7; ++i) {
    const float g0 = gs[0][i * 32 + kr], g1 = gs[1][i * 32 + kr];
#pragma unroll
    for (int j = 0; j < 4; ++j) {
      a0[2 * j] += g0 * lo2f(w[i][j]); a0[2 * j + 1] += g0 * hi2f(w[i][j]);
      a1[2 * j] += g1 * lo2f(w[i][j]); a1[2 * j + 1] += g1 * hi2f(w[i][j]);
    }
  }
#pragma unroll
  for (int j = 0; j < 8; ++j) {
    float a = a0[j], b = a1[j];
    a += __shfl_xor(a, 8); a += __shfl_xor(a, 16); a += __shfl_xor(a, 32);
    b += __shfl_xor(b, 8); b += __shfl_xor(b, 16); b += __shfl_xor(b, 32);
    if (lane < 8) { red[wid][0][lane * 8 + j] = a; red[wid][1][lane * 8 + j] = b; }
  }
  __syncthreads();
  float* yp = reinterpret_cast<float*>(ws + WS_YP);
  if (tid < 128) {
    const int r = tid >> 6, cc = tid & 63;
    atomicAdd(yp + (size_t)r * D_ + cg * 64 + cc, red[0][r][cc] + red[1][r][cc] + red[2][r][cc] + red[3][r][cc]);
  }
  asm volatile("s_waitcnt vmcnt(0)" ::: "memory");
  __syncthreads();
  if (tid == 0) {
    const unsigned t = atomicAdd(reinterpret_cast<unsigned*>(ws + WS_CNT) + cg, 1u);
    last = (t == 15u);
  }
  __syncthreads();
  if (last) {
    if (tid < 128) {
      const int r = tid >> 6, cc = tid & 63, n = cg * 64 + cc;
      float y = __hip_atomic_load(yp + (size_t)r * D_ + n, __ATOMIC_RELAXED, __HIP_MEMORY_SCOPE_AGENT);
      const int* sel = reinterpret_cast<const int*>(ws + WS_SEL);
      const int row = sel[r];
      const float rv = reinterpret_cast<const float*>(sel)[2 + r];
      const float sg = rbf(1.f / (1.f + __expf(-rv)));
      y = rbf(rbf(y) * sg);
      const float* rs = reinterpret_cast<const float*>(ws + WS_RS2);
      const float* mf = reinterpret_cast<const float*>(ws + WS_MODF);
      const u16* x1 = reinterpret_cast<const u16*>(ws + WS_X1);
      float m, rstd; row_stats2(rs, row, m, rstd);
      const float xv = bf2f(x1[(size_t)row * D_ + n]);
      const float h2 = ln2(xv, m, rstd, rbf(1.f + mf[4096 + n]), mf[3072 + n]);
      const float B = rbf(h2 + y);
      out[(size_t)row * D_ + n] = (u16)(pk2(xv + rbf(mf[5120 + n] * B), 0.f) & 0xffff);
    }
  }
}

extern "C" void kernel_launch(void* const* d_in, const int* in_sizes, int n_in, void* d_out, int out_size,
                              void* d_ws, size_t ws_size, hipStream_t stream) {
  const u16* x = (const u16*)d_in[0];
  const u16* wqkv = (const u16*)d_in[1];
  const u16* wphi = (const u16*)d_in[2];
  const u16* wo = (const u16*)d_in[3];
  const u16* wr = (const u16*)d_in[5];
  const u16* wg = (const u16*)d_in[6];
  const u16* wu = (const u16*)d_in[7];
  const u16* wd = (const u16*)d_in[8];
  const u16* c = (const u16*)d_in[9];
  const u16* wmod = (const u16*)d_in[10];
  char* ws = (char*)d_ws;
  u16* out = (u16*)d_out;
  hipFuncSetAttribute((const void*)k_qkv, hipFuncAttributeMaxDynamicSharedMemorySize, K1_LDS);
  k_prep<<<896, 256, 0, stream>>>(x, c, wmod, wqkv, wo, ws);
  k_qkv<<<256, 512, K1_LDS, stream>>>(x, wqkv, wphi, ws);
  k_o<<<257, 256, 0, stream>>>(wphi, wr, ws);
  k_wo<<<256, 512, 0, stream>>>(x, wo, ws);
  k_gateup<<<480, 256, 0, stream>>>(wg, wu, ws, out);
  k_down<<<256, 256, 0, stream>>>(wd, ws, out);
}
```
